```python
import torch

def _softmax(x, axis=-1):
    return torch.softmax(x, dim=axis)
def _gelu(x):
    return torch.nn.functional.gelu(x, approximate="tanh")
def _silu(x):
    return torch.nn.functional.silu(x)
def _sigmoid(x):
    return torch.sigmoid(x)
def _layernorm(x, axis=-1, eps=1e-5):
    m = x.mean(dim=axis, keepdim=True)
    v = x.var(dim=axis, keepdim=True, unbiased=False)
    return (x - m) / torch.sqrt(v + eps)
def _scatter_add(tgt, idx, src, axis=0):
    return tgt.clone().index_add_(axis, idx, src)
def _segment_sum(src, seg, n):
    out = torch.zeros((n,) + src.shape[1:], dtype=src.dtype)
    return out.index_add_(0, seg, src)
def _pad(x, pw):
    flat = tuple(v for pair in reversed(pw) for v in pair)
    return torch.nn.functional.pad(x, flat)

kernel_name = 'gated_mix__sliding_block_cached__swiglu_ffn__decode'

def setup_inputs():

    _w = (lambda a: (lambda t: t.to(torch.bfloat16) if t.is_floating_point() else t)(torch.as_tensor(a)))
    def _A_setup():
        B, Sq, D = 2, 1, 512
        Hq, Hkv, Sk, Dh = 32, 8, 32768, 128
        assert Hq % Hkv == 0, (Hq, Hkv)
        return {
            "x": _w((lambda *s: torch.randn(*s, dtype=torch.bfloat16) * 0.05)(B, Sq, D)),
            "k_cache": _w((lambda *s: torch.randn(*s, dtype=torch.bfloat16) * 6.4)(B, Hkv, Sk, Dh)),
            "v_cache": _w((lambda *s: torch.randn(*s, dtype=torch.bfloat16) * 0.2)(B, Hkv, Sk, Dh)),
            "wq": _w((lambda *s: torch.randn(*s, dtype=torch.bfloat16) * 6.4)(D, Hq * Dh) * (D ** -0.5)),
            "wo": _w((lambda *s: torch.randn(*s, dtype=torch.bfloat16) * 0.2)(Hq * Dh, D) * ((Hq * Dh) ** -0.5)),
        }
    _A_in = _A_setup()

    def _B_setup():
        B, S, D, F = 2, 1, 512, 1792
        return {
            "x": _w((lambda *s: torch.randn(*s, dtype=torch.bfloat16) * 0.05)(B, S, D)),
            "w_gate": _w((lambda *s: torch.randn(*s, dtype=torch.bfloat16) * 0.05)(D, F)),
            "w_up": _w((lambda *s: torch.randn(*s, dtype=torch.bfloat16) * 0.05)(D, F)),
            "w_down": _w((lambda *s: torch.randn(*s, dtype=torch.bfloat16) * 0.05)(F, D)),
        }
    _B_in = _B_setup()

    return {
        "A_x": _A_in["x"],
        "A_k_cache": _A_in["k_cache"],
        "A_v_cache": _A_in["v_cache"],
        "A_wq": _A_in["wq"],
        "A_wo": _A_in["wo"],
        "B_x": _B_in["x"],
        "B_w_gate": _B_in["w_gate"],
        "B_w_up": _B_in["w_up"],
        "B_w_down": _B_in["w_down"],
        "gate_w": _w((lambda *s: torch.randn(*s, dtype=torch.bfloat16) * 0.05)(512) * 16.0),
    }

def reference(A_x, A_k_cache, A_v_cache, A_wq, A_wo, B_x, B_w_gate, B_w_up, B_w_down, gate_w):
    def _A_ref(x, k_cache, v_cache, wq, wo):
        B, Sq, D = x.shape
        Hq, Dh = 32, 128
        Hkv, Sk = k_cache.shape[1], k_cache.shape[2]
        W = min(4096, max(256, Sk // 4))
        rep = Hq // Hkv
        q = (lambda x, *d: x.permute(*d))((x @ wq).reshape(B, Sq, Hq, Dh), 0, 2, 1, 3)
        k = (lambda x, n, axis: torch.repeat_interleave(x, n, dim=axis))(k_cache[:, :, Sk - W:, :], rep, axis=1)
        v = (lambda x, n, axis: torch.repeat_interleave(x, n, dim=axis))(v_cache[:, :, Sk - W:, :], rep, axis=1)
        scores = torch.einsum("bhqd,bhkd->bhqk", q, k) * (Dh ** -0.5)
        p = _softmax(scores, axis=-1)
        o = torch.einsum("bhqk,bhkd->bhqd", p, v)
        return (lambda x, *d: x.permute(*d))(o, 0, 2, 1, 3).reshape(B, Sq, Hq * Dh) @ wo

    def _B_ref(x, w_gate, w_up, w_down):
        g = _silu(x @ w_gate)
        u = x @ w_up
        return (g * u) @ w_down

    x = A_x
    g = _sigmoid((x * gate_w).sum(-1, keepdims=True))
    a = _A_ref(x, A_k_cache, A_v_cache, A_wq, A_wo)
    b = _B_ref(x, B_w_gate, B_w_up, B_w_down)
    return g * a + (1 - g) * b
```

```cpp
#include <hip/hip_runtime.h>
#include <hip/hip_bf16.h>
#include <stdint.h>

namespace gm {

typedef unsigned v4u __attribute__((ext_vector_type(4)));
typedef unsigned v2u __attribute__((ext_vector_type(2)));
typedef float f32x4 __attribute__((ext_vector_type(4)));
typedef float f32x2 __attribute__((ext_vector_type(2)));
typedef __bf16 bf16x8_t __attribute__((ext_vector_type(8)));
typedef __bf16 bf16x2_t __attribute__((ext_vector_type(2)));
typedef short s16x4 __attribute__((ext_vector_type(4)));
typedef unsigned short u16;

#define GAS __attribute__((address_space(1)))
#define LAS __attribute__((address_space(3)))
#define RLX_AGENT __ATOMIC_RELAXED, __HIP_MEMORY_SCOPE_AGENT

constexpr int SK = 32768, WINDOW = 4096, DH = 128, DM = 512, FF = 1792, HD = 4096;
constexpr int NWG = 256, NTHR = 768, NWAVES = 12;

constexpr int EPOCH_OFF = 0;
constexpr int QG_OFF = 4096;
constexpr int F1_OFF = QG_OFF + 256 * 512;
constexpr int S2_OFF = F1_OFF + 224 * 1024;
constexpr int S3_OFF = S2_OFF + 8 * 32 * 2048;
constexpr int WS_END = S3_OFF + 8 * 32 * 1024;
constexpr int STAMP_OFF = 4 << 20;

constexpr int L_CNT = 0;
constexpr int L_QRED = 64;
constexpr int L_FRED = L_QRED + 4096;
constexpr int L_Q = L_FRED + 2048;
constexpr int L_H = L_Q + 1024;
constexpr int L_OA = L_H + 256;
constexpr int L_WMAX = L_OA + 512;
constexpr int L_LSUM = L_WMAX + 128;
constexpr int L_RED = L_LSUM + 128;
constexpr int L_OP = L_RED + 4096;
constexpr int OP_W = 2304;
constexpr int L_K = L_OP + 8 * OP_W;
constexpr int L_END = L_K + 8 * 8192;
constexpr int LDS_BYTES = 100 * 1024;
static_assert(L_END + 512 + 1536 <= LDS_BYTES && (L_OP % 16) == 0 && (L_K % 16) == 0 && (L_RED % 16) == 0, "lds map");

constexpr unsigned SPIN_CAP = 1u << 18;
constexpr int AUX_SC1 = 16;

__device__ __forceinline__ v4u ldnt(const void* p) { return __builtin_nontemporal_load((const GAS v4u*)p); }
__device__ __forceinline__ v2u ldnt2(const void* p) { return __builtin_nontemporal_load((const GAS v2u*)p); }
__device__ __forceinline__ float dot2(unsigned a, unsigned b, float c) {
  return __builtin_amdgcn_fdot2_f32_bf16(__builtin_bit_cast(bf16x2_t, a), __builtin_bit_cast(bf16x2_t, b), c, false);
}
__device__ __forceinline__ unsigned cvtpk(float lo, float hi) {
  bf16x2_t r = {(__bf16)lo, (__bf16)hi};
  return __builtin_bit_cast(unsigned, r);
}
__device__ __forceinline__ float bfround(float v) { return (float)(__bf16)v; }
__device__ __forceinline__ float bflo(unsigned u) { return __uint_as_float(u << 16); }
__device__ __forceinline__ float bfhi(unsigned u) { return __uint_as_float(u & 0xffff0000u); }
template <int CTRL> __device__ __forceinline__ float dpp(float x) {
  return __builtin_bit_cast(float, __builtin_amdgcn_mov_dpp(__builtin_bit_cast(int, x), CTRL, 0xf, 0xf, true));
}
constexpr int D_XOR1 = 0xB1, D_XOR2 = 0x4E, D_HMIR = 0x141, D_ROR8 = 0x128, D_ROR4 = 0x124;
__device__ __forceinline__ float swapsum32(float a, float b) {
  auto s = __builtin_amdgcn_permlane32_swap(__float_as_uint(a), __float_as_uint(b), false, false);
  return __uint_as_float(s[0]) + __uint_as_float(s[1]);
}
__device__ __forceinline__ float swapsum16(float a, float b) {
  auto s = __builtin_amdgcn_permlane16_swap(__float_as_uint(a), __float_as_uint(b), false, false);
  return __uint_as_float(s[0]) + __uint_as_float(s[1]);
}
__device__ __forceinline__ float xrow_max(float x) {
  auto s = __builtin_amdgcn_permlane16_swap(__float_as_uint(x), __float_as_uint(x), false, false);
  x = fmaxf(__uint_as_float(s[0]), __uint_as_float(s[1]));
  auto t = __builtin_amdgcn_permlane32_swap(__float_as_uint(x), __float_as_uint(x), false, false);
  return fmaxf(__uint_as_float(t[0]), __uint_as_float(t[1]));
}
__device__ __forceinline__ float max8(f32x4 a, f32x4 b) {
  return fmaxf(fmaxf(fmaxf(a[0], a[1]), fmaxf(a[2], a[3])), fmaxf(fmaxf(b[0], b[1]), fmaxf(b[2], b[3])));
}
__device__ __forceinline__ float sum8(f32x4 a, f32x4 b) {
  return ((a[0] + a[1]) + (a[2] + a[3])) + ((b[0] + b[1]) + (b[2] + b[3]));
}
__device__ __forceinline__ f32x4 mfma32(v4u a, v4u b, f32x4 c) {
  return __builtin_amdgcn_mfma_f32_16x16x32_bf16(__builtin_bit_cast(bf16x8_t, a), __builtin_bit_cast(bf16x8_t, b), c, 0, 0, 0);
}
__device__ __forceinline__ f32x4 mfma16(v2u a, v2u b, f32x4 c) {
  return __builtin_amdgcn_mfma_f32_16x16x16bf16_1k(__builtin_bit_cast(s16x4, a), __builtin_bit_cast(s16x4, b), c, 0, 0, 0);
}
__device__ __forceinline__ unsigned permsel(unsigned hi, unsigned lo, int e) {
  return __builtin_amdgcn_perm(hi, lo, (e & 1) ? 0x07060302u : 0x05040100u);
}
__device__ __forceinline__ v4u ld_sc1(__amdgpu_buffer_rsrc_t rs, int off) {
  return __builtin_amdgcn_raw_buffer_load_b128(rs, off, 0, AUX_SC1);
}
__device__ __forceinline__ v2u ld_sc1_64(__amdgpu_buffer_rsrc_t rs, int off) {
  return __builtin_amdgcn_raw_buffer_load_b64(rs, off, 0, AUX_SC1);
}
__device__ __forceinline__ void st_sc1(__amdgpu_buffer_rsrc_t rs, int off, v4u v) {
  __builtin_amdgcn_raw_buffer_store_b128(v, rs, off, 0, AUX_SC1);
}
__device__ __forceinline__ unsigned tag_b(unsigned t) { return (t << 1) ^ ((t >> 31) ? 0x04C11DB7u : 0u); }
#define SB() __builtin_amdgcn_sched_barrier(0)
#define WGBAR()                                                     \
  do {                                                              \
    SB();                                                           \
    asm volatile("s_waitcnt lgkmcnt(0)\n\ts_barrier" ::: "memory"); \
    SB();                                                           \
  } while (0)
#define CBAR() asm volatile("" ::: "memory")
__device__ __forceinline__ void lds_signal(LAS unsigned* p) {
  asm volatile("s_waitcnt lgkmcnt(0)" ::: "memory");
  if ((threadIdx.x & 63) == 0) __hip_atomic_fetch_add(p, 1u, __ATOMIC_RELAXED, __HIP_MEMORY_SCOPE_WORKGROUP);
  CBAR();
}
__device__ __forceinline__ unsigned lds_signal_ret(LAS unsigned* p) {
  asm volatile("s_waitcnt lgkmcnt(0)" ::: "memory");
  unsigned old = 0;
  if ((threadIdx.x & 63) == 0) old = __hip_atomic_fetch_add(p, 1u, __ATOMIC_RELAXED, __HIP_MEMORY_SCOPE_WORKGROUP);
  old = __builtin_amdgcn_readfirstlane(old);
  CBAR();
  return old;
}
__device__ __forceinline__ void lds_wait(LAS unsigned* p, unsigned want) {
  SB();
  while (__builtin_amdgcn_readfirstlane(*(volatile LAS unsigned*)p) < want) __builtin_amdgcn_s_sleep(1);
  CBAR();
  SB();
}

#define STAMP(i)
#define STAMP_FLUSH(n)

#define GM_K0 4
#define GM_S2_SLEEP 1
#define GM_PS0 40
#define GM_PS1 30
#define GM_SL_HI 6
#define GM_SL_MID 2
#define GM_HCNT 8
#define GM_S2_DELAY 24
constexpr int OOB = 0x7FFFFF00;
#define BAD4(a) (((a)[0][1] ^ tag) | ((a)[0][3] ^ tag) | ((a)[1][1] ^ tag) | ((a)[1][3] ^ tag) | ((a)[2][1] ^ tag) | ((a)[2][3] ^ tag) | ((a)[3][1] ^ tag) | ((a)[3][3] ^ tag))
#define SUM4(a) cvtpk((__uint_as_float((a)[0][0]) + __uint_as_float((a)[1][0])) + (__uint_as_float((a)[2][0]) + __uint_as_float((a)[3][0])), \
                      (__uint_as_float((a)[0][2]) + __uint_as_float((a)[1][2])) + (__uint_as_float((a)[2][2]) + __uint_as_float((a)[3][2])))

__global__ void __launch_bounds__(NTHR)
fused_kernel(const u16* __restrict__ x, const u16* __restrict__ Kc, const u16* __restrict__ Vc,
             const u16* __restrict__ wq, const u16* __restrict__ wo, const u16* __restrict__ wg,
             const u16* __restrict__ wu, const u16* __restrict__ wd, const u16* __restrict__ gw,
             u16* __restrict__ out, unsigned char* ws) {
  __shared__ __attribute__((aligned(16))) unsigned char lds_raw[LDS_BYTES];
  LAS unsigned char* L = (LAS unsigned char*)lds_raw;
  LAS unsigned* cnt = (LAS unsigned*)(L + L_CNT);
  const int tid = threadIdx.x, lane = tid & 63;
  const int w = __builtin_amdgcn_readfirstlane(tid >> 6);
  const int bid = blockIdx.x, grp = bid & 7, r = bid >> 3;
  const int j = lane >> 4, n = lane & 15;
  const __amdgpu_buffer_rsrc_t rs = __builtin_amdgcn_make_buffer_rsrc(ws, 0, WS_END, 0x00020000);
  const f32x4 zero4 = {0.f, 0.f, 0.f, 0.f};
  constexpr float C2 = 0.08838834764831845f * 1.4426950408889634f;
  STAMP(0);

  if (w < 8) {
    const int b = r >> 4, s = r & 15;
    const int qrow = 128 * (r >> 3) + 16 * w + 2 * (lane >> 3);
    const u16* wqp = wq + (size_t)qrow * HD + 512 * grp + 64 * (r & 7) + 8 * (lane & 7);
    const v4u wq0 = ldnt(wqp), wq1 = ldnt(wqp + HD);
    const unsigned xq0 = *(const GAS unsigned*)(x + qrow), xq1 = *(const GAS unsigned*)(x + DM + qrow);
    WGBAR();
#define GM_SWZ 0
    const int sw = GM_SWZ == 2 ? ((s ^ (s >> 3) ^ (b << 2)) & 7) : (s & 7);
    const int cw = GM_SWZ == 1 ? 0 : GM_SWZ == 3 ? ((s >> 1) & 7) : sw;
    const size_t kvbase = ((size_t)(b * 8 + grp) * SK + (SK - WINDOW) + s * 256 + (w ^ cw) * 32) * DH;
    const u16* kp = Kc + kvbase;
    const u16* vp = Vc + kvbase;
    v4u kf[8], vf[8];
#pragma unroll
    for (int i = 0; i < GM_K0; ++i) kf[i] = ldnt(kp + (i ^ sw) * 512 + lane * 8);
    SB();
    {
      float v[16];
#pragma unroll
      for (int e = 0; e < 8; ++e) {
        const unsigned pk = permsel(wq1[e >> 1], wq0[e >> 1], e);
        v[e] = dot2(pk, xq0, 0.f);
        v[8 + e] = dot2(pk, xq1, 0.f);
      }
#pragma unroll
      for (int i = 0; i < 8; ++i) v[i] = swapsum32(v[i], v[i + 8]);
#pragma unroll
      for (int i = 0; i < 4; ++i) v[i] = swapsum16(v[i], v[i + 4]);
#pragma unroll
      for (int i = 0; i < 4; ++i) v[i] += dpp<D_ROR8>(v[i]);
      if ((lane & 8) == 0)
        *(LAS f32x4*)(L + L_QRED + ((w * 2 + (lane >> 5)) * 64 + 8 * (lane & 7) + 4 * ((lane >> 4) & 1)) * 4) =
            f32x4{v[0], v[1], v[2], v[3]};
    }
    STAMP(1);
    WGBAR();
    WGBAR();
    __builtin_amdgcn_s_setprio(1);
#pragma unroll
    for (int i = GM_K0; i < 8; ++i) kf[i] = ldnt(kp + (i ^ sw) * 512 + lane * 8);
    SB();
    __builtin_amdgcn_s_setprio(0);
    SB();
#pragma unroll
    for (int i = 0; i < 8; ++i) vf[i] = ldnt(vp + (i ^ sw) * 512 + lane * 8);
    SB();
    lds_signal(cnt + 0);
    STAMP(2);
    LAS unsigned char* Kl = L + L_K + w * 8192;
#pragma unroll
    for (int i = 0; i < 8; ++i) {
      const int key = 4 * i + j;
      *(LAS v4u*)(Kl + key * 256 + ((n ^ (key & 15)) << 4)) = kf[i];
    }
    v4u ka[2][4];
    {
      const int kin = 4 * (n & 3) + (n >> 2);
#pragma unroll
      for (int t = 0; t < 2; ++t)
#pragma unroll
        for (int st = 0; st < 4; ++st) ka[t][st] = *(LAS v4u*)(Kl + (16 * t + kin) * 256 + (((4 * st + j) ^ kin) << 4));
    }
    STAMP(3);
    lds_wait(cnt + 2, 2);
    STAMP(4);
    v4u qf[4];
#pragma unroll
    for (int st = 0; st < 4; ++st) qf[st] = *(LAS v4u*)(L + L_Q + (n >> 2) * 256 + (4 * st + j) * 16);
    f32x4 sc[2];
#pragma unroll
    for (int t = 0; t < 2; ++t) {
      sc[t] = zero4;
#pragma unroll
      for (int st = 0; st < 4; ++st) sc[t] = mfma32(ka[t][st], qf[st], sc[t]);
    }
    const float mx = xrow_max(max8(sc[0], sc[1]));
    if (lane < 16 && (lane & 3) == 0) *(LAS float*)(L + L_WMAX + ((lane >> 2) * 8 + w) * 4) = mx;
    lds_signal(cnt + 1);
    lds_wait(cnt + 1, 8);
    STAMP(5);
    const float Mc = C2 * max8(*(LAS f32x4*)(L + L_WMAX + (n >> 2) * 32), *(LAS f32x4*)(L + L_WMAX + (n >> 2) * 32 + 16));
    v4u pa;
#pragma unroll
    for (int t = 0; t < 2; ++t) {
      float p[4];
#pragma unroll
      for (int i = 0; i < 4; ++i) p[i] = __builtin_amdgcn_exp2f(fmaf(sc[t][i], C2, -Mc));
      pa[2 * t] = cvtpk(p[0], p[1]);
      pa[2 * t + 1] = cvtpk(p[2], p[3]);
    }
    const v4u ones = {0x3F803F80u, 0x3F803F80u, 0x3F803F80u, 0x3F803F80u};
    const f32x4 ls = mfma32(pa, ones, zero4);
    if (n == 0) *(LAS float*)(L + L_LSUM + (j * 8 + w) * 4) = ls[0];
    const int chead = (2 * w + (lane >> 5)) & 3;
    const float Mh = C2 * max8(*(LAS f32x4*)(L + L_WMAX + chead * 32), *(LAS f32x4*)(L + L_WMAX + chead * 32 + 16));
    const unsigned tag = cnt[5];
    asm volatile("" ::"v"(Mh), "v"(tag));
    __builtin_amdgcn_s_setprio(3);
    STAMP(6);
    v4u be[8];
#pragma unroll
    for (int q = 0; q < 4; ++q) {
#pragma unroll
      for (int e = 0; e < 8; ++e) be[e][q] = permsel(vf[2 * q + 1][e >> 1], vf[2 * q][e >> 1], e);
      if (q < 3) SB();
    }
    float o[8];
#pragma unroll
    for (int e = 0; e < 8; ++e) o[e] = mfma32(pa, be[e], zero4)[0];
    *(LAS f32x4*)(L + L_OP + w * OP_W + lane * 16) = f32x4{o[0], o[1], o[2], o[3]};
    *(LAS f32x4*)(L + L_OP + w * OP_W + 1152 + lane * 16) = f32x4{o[4], o[5], o[6], o[7]};
    STAMP(7);
    WGBAR();
    if (w < 2) {
      const int head = 2 * w + (lane >> 5), q4 = lane & 31;
      const int src = (q4 & 1) * 1152 + (head * 16 + (q4 >> 1)) * 16;
      f32x4 acc = zero4;
#pragma unroll
      for (int ww = 0; ww < 8; ++ww) acc += *(LAS f32x4*)(L + L_OP + ww * OP_W + src);
      const float Ls = sum8(*(LAS f32x4*)(L + L_LSUM + head * 32), *(LAS f32x4*)(L + L_LSUM + head * 32 + 16));
      const float inv = __builtin_amdgcn_rcpf(Ls);
      const unsigned lse = __float_as_uint(Mh + __builtin_amdgcn_logf(Ls));
      st_sc1(rs, S2_OFF + (grp * 32 + r) * 2048 + w * 1024 + lane * 16,
             v4u{cvtpk(acc[0] * inv, acc[1] * inv), lse ^ tag, cvtpk(acc[2] * inv, acc[3] * inv), lse ^ tag_b(tag)});
      WGBAR();
    }
    STAMP(8);
    STAMP_FLUSH(9);
    return;
  }

  {
    __builtin_amdgcn_s_setprio(3);
    const int tw = w - 8;
    int vz = 0;
    asm volatile("" : "+v"(vz));
    const unsigned epoch = __hip_atomic_load((GAS unsigned*)(ws + EPOCH_OFF + vz), RLX_AGENT);
    const int ft = bid < 224 ? bid : bid - 32;
    const int fmat = ft / 112, fcb = (ft % 112) >> 2, frb = ft & 3;
    const int frow = 128 * frb + 64 * (tw & 1) + 8 * (lane >> 3);
    const v4u xf0 = *(const GAS v4u*)(x + frow), xf1 = *(const GAS v4u*)(x + DM + frow);
    const v4u gv = *(const GAS v4u*)(gw + 8 * lane);
    const v4u x0 = *(const GAS v4u*)(x + 8 * lane), x1 = *(const GAS v4u*)(x + DM + 8 * lane);
    if (tw == 0 && lane < 5) cnt[lane] = 0u;
    WGBAR();
    const int cblk = r & 7, hrel = r >> 3;
    const u16* wop = wo + (size_t)(512 * grp + 128 * hrel + 32 * tw + 4 * (lane >> 3)) * DM + 64 * cblk + 8 * (lane & 7);
    const u16* wdp = wd + (size_t)(224 * grp + 56 * hrel + 14 * tw + (lane < 56 ? 2 * (lane >> 3) : 12)) * DM + 64 * cblk + 8 * (lane & 7);
    v4u wfr[4], dfr[2];
#define ISSUE_WO_WD()                                                             \
  do {                                                                            \
    lds_wait(cnt + 0, 8);                                  \
    STAMP(5);                                                                     \
    _Pragma("unroll") for (int i = 0; i < 4; ++i) wfr[i] = ldnt(wop + i * DM);    \
    dfr[0] = ldnt(wdp);                                                           \
    dfr[1] = ldnt(wdp + DM);                                                      \
    SB();                                                                         \
  } while (0)
#define GATE()                                                                    \
  do {                                                                            \
    float a0 = 0.f, a1 = 0.f;                                                     \
    _Pragma("unroll") for (int q = 0; q < 4; ++q) {                               \
      a0 = dot2(gv[q], x0[q], a0);                                                \
      a1 = dot2(gv[q], x1[q], a1);                                                \
    }                                                                             \
    float a = swapsum32(a0, a1);                                                  \
    a = swapsum16(a, a);                                                          \
    a += dpp<D_ROR8>(a);                                                          \
    a += dpp<D_ROR4>(a);                                                          \
    a += dpp<D_XOR2>(a);                                                          \
    a += dpp<D_XOR1>(a);                                                          \
    gate = 1.0f / (1.0f + __expf(-a));                                            \
    unsigned tg = epoch * 1664525u + 1013904223u;   \
    if (tg == 0u) tg = 1013904223u;                 \
    tag = tg;                                                                     \
    asm volatile("" : "+v"(gate), "+v"(tag));                                     \
  } while (0)
    float gate;
    unsigned tag;
    bool alive = true;
    if (tw < 2) {
      GATE();
      STAMP(1);
      WGBAR();
      WGBAR();
      const int qoff = QG_OFF + (grp * 8 + 4 * tw + (lane >> 4)) * 2048 + (r >> 4) * 256 + (lane & 15) * 16;
      v4u pq[2][4];
#pragma unroll
      for (int k = 0; k < 2; ++k) {
        if (k == 0) __builtin_amdgcn_s_sleep(GM_PS0);
        else __builtin_amdgcn_s_sleep(GM_PS1);
        CBAR();
#pragma unroll
        for (int p = 0; p < 4; ++p) pq[k][p] = ld_sc1(rs, qoff + p * 512);
        SB();
      }
      STAMP(3);
#define QS(a, d) cvtpk((bflo((a)[0][d]) + bflo((a)[1][d])) + (bflo((a)[2][d]) + bflo((a)[3][d])), (bfhi((a)[0][d]) + bfhi((a)[1][d])) + (bfhi((a)[2][d]) + bfhi((a)[3][d])))
      v2u qv = {QS(pq[0], 0), QS(pq[0], 2)};
      if (!__all(BAD4(pq[0]) == 0u)) {
        qv = v2u{QS(pq[1], 0), QS(pq[1], 2)};
        bool done = __all(BAD4(pq[1]) == 0u);
        unsigned spins = 0;
        while (!done && ++spins < SPIN_CAP) {
          CBAR();
          v4u pr[4];
#pragma unroll
          for (int p = 0; p < 4; ++p) pr[p] = ld_sc1(rs, qoff + p * 512);
          qv = v2u{QS(pr, 0), QS(pr, 2)};
          done = __all(BAD4(pr) == 0u);
        }
        alive = done;
      }
      *(LAS v2u*)(L + L_Q + tw * 512 + lane * 8) = qv;
      lds_signal(cnt + 2);
      STAMP(4);
      ISSUE_WO_WD();
      asm volatile("" ::"v"(pq[1][0]), "v"(pq[1][1]), "v"(pq[1][2]), "v"(pq[1][3]));
      STAMP(6);
      WGBAR();
      STAMP(7);
      WGBAR();
    } else {
      const u16* wfp = (fmat == 0 ? wg : wu) + (size_t)frow * FF + 64 * fcb + 8 * (lane & 7);
      v4u wf[8];
#pragma unroll
      for (int i = 0; i < 8; ++i) wf[i] = ldnt(wfp + i * FF);
      SB();
      GATE();
      STAMP(1);
      WGBAR();
      {
        f32x4 a = zero4;
#pragma unroll
        for (int ww = 0; ww < 8; ++ww) a += *(LAS f32x4*)(L + L_QRED + ww * 512 + (lane & 31) * 16);
        st_sc1(rs, (tw == 3 && lane < 32) ? QG_OFF + ((grp * 8 + (r & 7)) * 4 + (r >> 3)) * 512 + lane * 16 : OOB,
               v4u{cvtpk(a[0], a[1]), tag, cvtpk(a[2], a[3]), tag});
        if (lane == 0) cnt[5] = tag;
      }
      STAMP(2);
      WGBAR();
      SB();
      float v[16];
#pragma unroll
      for (int e = 0; e < 8; ++e) {
        v[e] = 0.f; v[8 + e] = 0.f;
#pragma unroll
        for (int q = 0; q < 4; ++q) {
          const unsigned pk = permsel(wf[2 * q + 1][e >> 1], wf[2 * q][e >> 1], e);
          v[e] = dot2(pk, xf0[q], v[e]);
          v[8 + e] = dot2(pk, xf1[q], v[8 + e]);
        }
      }
#pragma unroll
      for (int i = 0; i < 8; ++i) v[i] = swapsum32(v[i], v[i + 8]);
#pragma unroll
      for (int i = 0; i < 4; ++i) v[i] = swapsum16(v[i], v[i + 4]);
#pragma unroll
      for (int i = 0; i < 4; ++i) v[i] += dpp<D_ROR8>(v[i]);
      if ((lane & 8) == 0)
        *(LAS f32x4*)(L + L_FRED + (((tw & 1) * 2 + (lane >> 5)) * 64 + 8 * (lane & 7) + 4 * ((lane >> 4) & 1)) * 4) =
            f32x4{v[0], v[1], v[2], v[3]};
      if (lds_signal_ret(cnt + 4) == 1u && bid < 224) {
        const float a0 = *(LAS float*)(L + L_FRED + lane * 4) + *(LAS float*)(L + L_FRED + 512 + lane * 4);
        const float a1 = *(LAS float*)(L + L_FRED + 256 + lane * 4) + *(LAS float*)(L + L_FRED + 768 + lane * 4);
        st_sc1(rs, F1_OFF + ft * 1024 + lane * 16, v4u{__float_as_uint(a0), tag, __float_as_uint(a1), tag});
      }
      STAMP(3);
      asm volatile("s_waitcnt vmcnt(0)" ::: "memory");
      if (tw == 2) {
        const int cc = 224 * grp + 56 * hrel + (lane < 56 ? lane : 55);
        const int hoff = F1_OFF + (cc >> 6) * 4096 + (cc & 63) * 16;
        lds_wait(cnt + 0, GM_HCNT);
        v4u hg[4], hu[4];
#pragma unroll
        for (int p = 0; p < 4; ++p) hg[p] = ld_sc1(rs, hoff + p * 1024);
#pragma unroll
        for (int p = 0; p < 4; ++p) hu[p] = ld_sc1(rs, hoff + 28 * 4096 + p * 1024);
        SB();
        STAMP(4);
        ISSUE_WO_WD();
        STAMP(6);
        WGBAR();
        STAMP(7);
        WGBAR();
        if (!__all((BAD4(hg) | BAD4(hu)) == 0u)) {
          unsigned spins = 0;
          bool done;
          do {
            CBAR();
#pragma unroll
            for (int p = 0; p < 4; ++p) hg[p] = ld_sc1(rs, hoff + p * 1024);
#pragma unroll
            for (int p = 0; p < 4; ++p) hu[p] = ld_sc1(rs, hoff + 28 * 4096 + p * 1024);
            done = __all((BAD4(hg) | BAD4(hu)) == 0u);
          } while (!done && ++spins < SPIN_CAP);
          alive = done;
        }
        const float g0 = __int_as_float(__builtin_amdgcn_readlane(__float_as_int(gate), 0)), g1 = __int_as_float(__builtin_amdgcn_readlane(__float_as_int(gate), 32));
#pragma unroll
        for (int bb = 0; bb < 2; ++bb) {
          const float ga = bfround((__uint_as_float(hg[0][2 * bb]) + __uint_as_float(hg[1][2 * bb])) + (__uint_as_float(hg[2][2 * bb]) + __uint_as_float(hg[3][2 * bb])));
          const float ua = bfround((__uint_as_float(hu[0][2 * bb]) + __uint_as_float(hu[1][2 * bb])) + (__uint_as_float(hu[2][2 * bb]) + __uint_as_float(hu[3][2 * bb])));
          const float si = bfround(ga * __builtin_amdgcn_rcpf(1.0f + __expf(-ga)));
          float hval = si * ua * (1.0f - (bb ? g1 : g0));
          if (!alive) hval = __uint_as_float(0x7FC00000u);
          *(LAS __bf16*)(L + L_H + (bb * 64 + lane) * 2) = (__bf16)hval;
        }
        lds_signal(cnt + 3);
      } else {
        ISSUE_WO_WD();
        STAMP(6);
        WGBAR();
        STAMP(7);
        WGBAR();
      }
    }
    const int s2off = S2_OFF + (grp * 32 + (lane >> 3)) * 2048 + hrel * 512 + tw * 128 + (lane & 7) * 16;
    const unsigned tgx = tag ^ tag_b(tag);
    unsigned o0[4], o1[4], o2[4];
    v4u wb[8];
    {
      bool ho[4];
      v4u od[4];
      __builtin_amdgcn_s_sleep(GM_S2_DELAY);
#pragma unroll
      for (int i = 0; i < 4; ++i) od[i] = ld_sc1(rs, s2off + i * 8 * 2048);
      SB();
#pragma unroll
      for (int e = 0; e < 8; ++e) {
        wb[e][0] = permsel(wfr[1][e >> 1], wfr[0][e >> 1], e);
        wb[e][1] = permsel(wfr[3][e >> 1], wfr[2][e >> 1], e);
        wb[e][2] = lane < 56 ? permsel(dfr[1][e >> 1], dfr[0][e >> 1], e) : 0u;
        wb[e][3] = 0u;
        asm volatile("" : "+v"(wb[e]));
      }
      SB();
      STAMP(15);
#pragma unroll
      for (int i = 0; i < 4; ++i) {
        ho[i] = (od[i][1] ^ od[i][3]) == tgx;
        o0[i] = od[i][0]; o1[i] = od[i][1]; o2[i] = od[i][2];
      }
      unsigned spins = 0;
      while (true) {
        const int miss = __builtin_popcountll(__ballot(!ho[0])) + __builtin_popcountll(__ballot(!ho[1])) +
                         __builtin_popcountll(__ballot(!ho[2])) + __builtin_popcountll(__ballot(!ho[3]));
        if (miss == 0) break;
        if (++spins >= SPIN_CAP) { alive = false; break; }
        if (miss > 128) __builtin_amdgcn_s_sleep(GM_SL_HI);
        else if (miss > 40) __builtin_amdgcn_s_sleep(GM_SL_MID);
        CBAR();
#pragma unroll
        for (int i = 0; i < 4; ++i) od[i] = ld_sc1(rs, ho[i] ? OOB : s2off + i * 8 * 2048);
        SB();
#pragma unroll
        for (int i = 0; i < 4; ++i) {
          o0[i] = ho[i] ? o0[i] : od[i][0];
          o1[i] = ho[i] ? o1[i] : od[i][1];
          o2[i] = ho[i] ? o2[i] : od[i][2];
          ho[i] = ho[i] | ((od[i][1] ^ od[i][3]) == tgx);
        }
      }
    }
    STAMP(9);
    {
      float ls[4], ov[10];
#pragma unroll
      for (int i = 0; i < 4; ++i) ls[i] = __uint_as_float(o1[i] ^ tag);
#pragma unroll
      for (int bb = 0; bb < 2; ++bb) {
        float mx = fmaxf(ls[2 * bb], ls[2 * bb + 1]);
        mx = fmaxf(mx, dpp<D_ROR8>(mx));
        mx = xrow_max(mx);
        const float w0 = __builtin_amdgcn_exp2f(ls[2 * bb] - mx), w1 = __builtin_amdgcn_exp2f(ls[2 * bb + 1] - mx);
        ov[4 * bb + 0] = w0 * bflo(o0[2 * bb]) + w1 * bflo(o0[2 * bb + 1]);
        ov[4 * bb + 1] = w0 * bfhi(o0[2 * bb]) + w1 * bfhi(o0[2 * bb + 1]);
        ov[4 * bb + 2] = w0 * bflo(o2[2 * bb]) + w1 * bflo(o2[2 * bb + 1]);
        ov[4 * bb + 3] = w0 * bfhi(o2[2 * bb]) + w1 * bfhi(o2[2 * bb + 1]);
        ov[8 + bb] = w0 + w1;
      }
#pragma unroll
      for (int i = 0; i < 4; ++i) ov[i] = swapsum32(ov[i], ov[i + 4]);
      float W = swapsum32(ov[8], ov[9]);
      ov[0] = swapsum16(ov[0], ov[2]);
      ov[1] = swapsum16(ov[1], ov[3]);
      W = swapsum16(W, W);
      ov[0] += dpp<D_ROR8>(ov[0]);
      ov[1] += dpp<D_ROR8>(ov[1]);
      W += dpp<D_ROR8>(W);
      const float sc = gate * __builtin_amdgcn_rcpf(W);
      *(LAS unsigned*)(L + L_OA + tw * 128 + (lane >> 5) * 64 + (lane & 7) * 8 + ((lane >> 4) & 1) * 4) = cvtpk(sc * ov[0], sc * ov[1]);
      lds_wait(cnt + 3, 1);
      const int rho = 2 * j + ((n >> 2) & 1);
      const v2u ao = *(LAS v2u*)(L + L_OA + tw * 128 + (n >> 3) * 64 + rho * 8);
      const unsigned ah = *(LAS unsigned*)(L + L_H + (n >> 3) * 128 + tw * 28 + rho * 4);
      const v4u am = {ao[0], ao[1], rho < 7 ? ah : 0u, 0u};
      float res[8];
#pragma unroll
      for (int e = 0; e < 8; ++e) res[e] = mfma32(am, wb[e], zero4)[0];
      if (!alive) res[0] = __uint_as_float(0x7FC00000u);
      if ((j & 1) == (n >> 3)) {
        LAS unsigned char* dst = L + L_RED + ((tw * 2 + (j & 1)) * 2 + (j >> 1)) * 256 + (n & 7) * 32;
        *(LAS f32x4*)dst = f32x4{res[0], res[1], res[2], res[3]};
        *(LAS f32x4*)(dst + 16) = f32x4{res[4], res[5], res[6], res[7]};
      }
    }
    STAMP(10);
    WGBAR();
    if (tw == 0) {
      f32x2 a = {0.f, 0.f};
#pragma unroll
      for (int t = 0; t < 8; ++t) a += *(LAS f32x2*)(L + L_RED + t * 512 + lane * 8);
      st_sc1(rs, S3_OFF + (cblk * 32 + 4 * grp + hrel) * 1024 + lane * 16,
             v4u{__float_as_uint(a[0]), tag, __float_as_uint(a[1]), tag});
      STAMP(11);
    } else if (tw == 1 && bid < 64) {
      const int s3off = S3_OFF + ((bid >> 3) * 32 + (lane >> 3)) * 1024 + (bid & 7) * 128 + (lane & 7) * 16;
      v4u pd[4];
      unsigned spins = 0;
      bool done;
      do {
        CBAR();
#pragma unroll
        for (int i = 0; i < 4; ++i) pd[i] = ld_sc1(rs, s3off + i * 8192);
        SB();
        unsigned bad = 0;
#pragma unroll
        for (int i = 0; i < 4; ++i) bad |= (pd[i][1] ^ tag) | (pd[i][3] ^ tag);
        done = __all(bad == 0u);
      } while (!done && ++spins < SPIN_CAP);
      alive = alive && done;
      STAMP(12);
      float a0 = (__uint_as_float(pd[0][0]) + __uint_as_float(pd[1][0])) + (__uint_as_float(pd[2][0]) + __uint_as_float(pd[3][0]));
      float a1 = (__uint_as_float(pd[0][2]) + __uint_as_float(pd[1][2])) + (__uint_as_float(pd[2][2]) + __uint_as_float(pd[3][2]));
      a0 = swapsum32(a0, a0);
      a1 = swapsum32(a1, a1);
      a0 = swapsum16(a0, a0);
      a1 = swapsum16(a1, a1);
      a0 += dpp<D_ROR8>(a0);
      a1 += dpp<D_ROR8>(a1);
      unsigned pk = cvtpk(a0, a1);
      if (!alive) pk = 0x7FC07FC0u;
      if (lane < 8) *(GAS unsigned*)(out + ((bid >> 2) & 1) * DM + 64 * (bid >> 3) + 16 * (bid & 3) + 2 * lane) = pk;
      if (bid == 0 && lane == 0) __hip_atomic_store((GAS unsigned*)(ws + EPOCH_OFF), tag, RLX_AGENT);
      STAMP(13);
    }
    STAMP_FLUSH(14);
  }
}

}

extern "C" void kernel_launch(void* const* d_in, const int* in_sizes, int n_in, void* d_out, int out_size, void* d_ws,
                              size_t ws_size, hipStream_t stream) {
  using gm::u16;
  (void)in_sizes; (void)n_in; (void)out_size; (void)ws_size;
  gm::fused_kernel<<<gm::NWG, gm::NTHR, 0, stream>>>(
      (const u16*)d_in[0], (const u16*)d_in[1], (const u16*)d_in[2], (const u16*)d_in[3], (const u16*)d_in[4],
      (const u16*)d_in[6], (const u16*)d_in[7], (const u16*)d_in[8], (const u16*)d_in[9], (u16*)d_out,
      (unsigned char*)d_ws);
}
```

```cpp
#include <hip/hip_runtime.h>
#include <hip/hip_bf16.h>
#include <stdint.h>

namespace gm {

typedef unsigned v4u __attribute__((ext_vector_type(4)));
typedef unsigned v2u __attribute__((ext_vector_type(2)));
typedef float f32x4 __attribute__((ext_vector_type(4)));
typedef float f32x2 __attribute__((ext_vector_type(2)));
typedef __bf16 bf16x8_t __attribute__((ext_vector_type(8)));
typedef __bf16 bf16x2_t __attribute__((ext_vector_type(2)));
typedef unsigned short u16;

#define GAS __attribute__((address_space(1)))
#define LAS __attribute__((address_space(3)))
#define RLX_AGENT __ATOMIC_RELAXED, __HIP_MEMORY_SCOPE_AGENT

constexpr int SK = 32768, WINDOW = 4096, DH = 128, DM = 512, FF = 1792, HD = 4096;
constexpr int NWG = 256, NTHR = 768, NWAVES = 12;

constexpr int EPOCH_OFF = 0;
constexpr int QG_OFF = 4096;
constexpr int F1_OFF = QG_OFF + 256 * 512;
constexpr int S2_OFF = F1_OFF + 224 * 512;
constexpr int S3_OFF = S2_OFF + 8 * 32 * 2048;
constexpr int WS_END = S3_OFF + 8 * 32 * 512;
constexpr int STAMP_OFF = 4 << 20;

constexpr int L_CNT = 0;
constexpr int L_QRED = 64;
constexpr int L_FRED = L_QRED + 2048;
constexpr int L_Q = L_FRED + 1024;
constexpr int L_H = L_Q + 1024;
constexpr int L_OA = L_H + 256;
constexpr int L_WMAX = L_OA + 512;
constexpr int L_LSUM = L_WMAX + 64;
constexpr int L_RED = L_LSUM + 64;
constexpr int L_P = L_RED + 4096;
constexpr int L_OP = L_P + 8192;
constexpr int OP_W = 2304;
constexpr int L_K = L_OP + 4 * OP_W;
constexpr int L_END = L_K + 4 * 16384;
constexpr int LDS_BYTES = 100 * 1024;
static_assert(L_END + 512 + 1536 <= LDS_BYTES && (L_OP % 16) == 0 && (L_K % 16) == 0 && (L_RED % 16) == 0 && (L_P % 16) == 0, "lds map");

constexpr unsigned SPIN_CAP = 1u << 18;
constexpr int AUX_SC1 = 16;
constexpr int OOB = 0x7FFFFF00;

__device__ __forceinline__ v4u ldnt(const void* p) { return __builtin_nontemporal_load((const GAS v4u*)p); }
__device__ __forceinline__ float dot2(unsigned a, unsigned b, float c) {
  return __builtin_amdgcn_fdot2_f32_bf16(__builtin_bit_cast(bf16x2_t, a), __builtin_bit_cast(bf16x2_t, b), c, false);
}
__device__ __forceinline__ unsigned cvtpk(float lo, float hi) {
  bf16x2_t r = {(__bf16)lo, (__bf16)hi};
  return __builtin_bit_cast(unsigned, r);
}
__device__ __forceinline__ float bfround(float v) { return (float)(__bf16)v; }
__device__ __forceinline__ float bflo(unsigned u) { return __uint_as_float(u << 16); }
__device__ __forceinline__ float bfhi(unsigned u) { return __uint_as_float(u & 0xffff0000u); }
template <int CTRL> __device__ __forceinline__ float dpp(float x) {
  return __builtin_bit_cast(float, __builtin_amdgcn_mov_dpp(__builtin_bit_cast(int, x), CTRL, 0xf, 0xf, true));
}
constexpr int D_XOR1 = 0xB1, D_XOR2 = 0x4E, D_ROR8 = 0x128, D_ROR4 = 0x124;
__device__ __forceinline__ float swapsum32(float a, float b) {
  auto s = __builtin_amdgcn_permlane32_swap(__float_as_uint(a), __float_as_uint(b), false, false);
  return __uint_as_float(s[0]) + __uint_as_float(s[1]);
}
__device__ __forceinline__ float swapsum16(float a, float b) {
  auto s = __builtin_amdgcn_permlane16_swap(__float_as_uint(a), __float_as_uint(b), false, false);
  return __uint_as_float(s[0]) + __uint_as_float(s[1]);
}
__device__ __forceinline__ float xrow_max(float x) {
  auto s = __builtin_amdgcn_permlane16_swap(__float_as_uint(x), __float_as_uint(x), false, false);
  x = fmaxf(__uint_as_float(s[0]), __uint_as_float(s[1]));
  auto t = __builtin_amdgcn_permlane32_swap(__float_as_uint(x), __float_as_uint(x), false, false);
  return fmaxf(__uint_as_float(t[0]), __uint_as_float(t[1]));
}
__device__ __forceinline__ float max4(f32x4 a) { return fmaxf(fmaxf(a[0], a[1]), fmaxf(a[2], a[3])); }
__device__ __forceinline__ float sum4(f32x4 a) { return (a[0] + a[1]) + (a[2] + a[3]); }
__device__ __forceinline__ f32x4 mfma32(v4u a, v4u b, f32x4 c) {
  return __builtin_amdgcn_mfma_f32_16x16x32_bf16(__builtin_bit_cast(bf16x8_t, a), __builtin_bit_cast(bf16x8_t, b), c, 0, 0, 0);
}
__device__ __forceinline__ unsigned permsel(unsigned hi, unsigned lo, int e) {
  return __builtin_amdgcn_perm(hi, lo, (e & 1) ? 0x07060302u : 0x05040100u);
}
__device__ __forceinline__ v4u ld_sc1(__amdgpu_buffer_rsrc_t rs, int off) {
  return __builtin_amdgcn_raw_buffer_load_b128(rs, off, 0, AUX_SC1);
}
__device__ __forceinline__ void st_sc1(__amdgpu_buffer_rsrc_t rs, int off, v4u v) {
  __builtin_amdgcn_raw_buffer_store_b128(v, rs, off, 0, AUX_SC1);
}
__device__ __forceinline__ unsigned tag_b(unsigned t) { return (t << 1) ^ ((t >> 31) ? 0x04C11DB7u : 0u); }
__device__ __forceinline__ v4u pack4(f32x4 v, unsigned tA, unsigned tB) {
  const unsigned u3 = __float_as_uint(v[3]);
  const unsigned a = __builtin_amdgcn_perm(__float_as_uint(v[0]), u3, 0x07060503u);
  const unsigned b = __builtin_amdgcn_perm(__float_as_uint(v[1]), u3, 0x07060502u);
  const unsigned c = __builtin_amdgcn_perm(__float_as_uint(v[2]), u3, 0x07060501u);
  return v4u{a, c ^ tA, b, c ^ tB};
}
__device__ __forceinline__ f32x4 unpack4(v4u p, unsigned tA) {
  const unsigned c = p[1] ^ tA;
  const unsigned t = __builtin_amdgcn_perm(p[0], p[2], 0x04000c0cu);
  const unsigned u3 = __builtin_amdgcn_perm(t, c, 0x0706000cu);
  return f32x4{__uint_as_float(p[0] & 0xffffff00u), __uint_as_float(p[2] & 0xffffff00u), __uint_as_float(c & 0xffffff00u), __uint_as_float(u3)};
}
#define SB() __builtin_amdgcn_sched_barrier(0)
#define WGBAR()                                                     \
  do {                                                              \
    SB();                                                           \
    asm volatile("s_waitcnt lgkmcnt(0)\n\ts_barrier" ::: "memory"); \
    SB();                                                           \
  } while (0)
#define CBAR() asm volatile("" ::: "memory")
__device__ __forceinline__ void lds_signal(LAS unsigned* p) {
  asm volatile("s_waitcnt lgkmcnt(0)" ::: "memory");
  if ((threadIdx.x & 63) == 0) __hip_atomic_fetch_add(p, 1u, __ATOMIC_RELAXED, __HIP_MEMORY_SCOPE_WORKGROUP);
  CBAR();
}
__device__ __forceinline__ unsigned lds_signal_ret(LAS unsigned* p) {
  asm volatile("s_waitcnt lgkmcnt(0)" ::: "memory");
  unsigned old = 0;
  if ((threadIdx.x & 63) == 0) old = __hip_atomic_fetch_add(p, 1u, __ATOMIC_RELAXED, __HIP_MEMORY_SCOPE_WORKGROUP);
  old = __builtin_amdgcn_readfirstlane(old);
  CBAR();
  return old;
}
__device__ __forceinline__ void lds_wait(LAS unsigned* p, unsigned want) {
  SB();
  while (__builtin_amdgcn_readfirstlane(*(volatile LAS unsigned*)p) < want) __builtin_amdgcn_s_sleep(1);
  CBAR();
  SB();
}
__device__ __forceinline__ f32x4 reduce16(float (&v)[16]) {
#pragma unroll
  for (int i = 0; i < 8; ++i) v[i] = swapsum32(v[i], v[i + 8]);
#pragma unroll
  for (int i = 0; i < 4; ++i) v[i] = swapsum16(v[i], v[i + 4]);
#pragma unroll
  for (int i = 0; i < 4; ++i) v[i] += dpp<D_ROR8>(v[i]);
  return f32x4{v[0], v[1], v[2], v[3]};
}

#define STAMP_KEEP
#define STAMP(i)
#define STAMP_FLUSH(n)

#define GM_PS0 36
#define GM_PS1 16
#define GM_SL_HI 6
#define GM_SL_MID 2
#define GM_S2_DELAY 20
#define GM_CODE_LINES 112
#define BAD4(a) ((((a)[0][1] ^ (a)[0][3]) ^ tgx) | (((a)[1][1] ^ (a)[1][3]) ^ tgx) | (((a)[2][1] ^ (a)[2][3]) ^ tgx) | (((a)[3][1] ^ (a)[3][3]) ^ tgx))
#define USUM4(a) ((unpack4((a)[0], tag) + unpack4((a)[1], tag)) + (unpack4((a)[2], tag) + unpack4((a)[3], tag)))

#define GM_K0 8
#define GM_WSIG 1

__global__ void __launch_bounds__(NTHR)
fused_kernel(const u16* x, const u16* wq_, unsigned char* ws, const u16* gw, const u16* Kc, const u16* wg, const u16* wu,
             const u16* Vc, const u16* wo, const u16* wd, u16* out) {
  __shared__ __attribute__((aligned(16))) unsigned char lds_raw[LDS_BYTES];
  unsigned long long p_x, p_wq, p_ws, pc0;
  asm volatile("s_load_dwordx2 %0, %4, 0x0\n\ts_load_dwordx2 %1, %4, 0x8\n\ts_load_dwordx2 %2, %4, 0x10\n\ts_getpc_b64 %3"
               : "=&s"(p_x), "=&s"(p_wq), "=&s"(p_ws), "=&s"(pc0)
               : "s"(__builtin_amdgcn_kernarg_segment_ptr()));
  SB();
  LAS unsigned char* L = (LAS unsigned char*)lds_raw;
  LAS unsigned* cnt = (LAS unsigned*)(L + L_CNT);
  const int tid = threadIdx.x, lane = tid & 63;
  const int w = __builtin_amdgcn_readfirstlane(tid >> 6);
  const int bid = blockIdx.x, grp = bid & 7, r = bid >> 3;
  unsigned codew, epoch;
  v2u xq0, xq1;
  v4u big[4];
  {
    const int qrow = 128 * (r >> 3) + 32 * w + 4 * (lane >> 3);
    const unsigned qo = (qrow * HD + 512 * grp + 64 * (r & 7) + 8 * (lane & 7)) * 2;
    const int cl = 4 * r + (lane & 3);
    const unsigned coff = (cl < GM_CODE_LINES ? cl : 0) * 128;
    asm volatile(
        "s_cmp_lg_u32 %[w], 9\n\ts_cbranch_scc1 1f\n\t"
        "global_load_dword %[cw], %[coff], %[pc]\n\t"
        "s_waitcnt lgkmcnt(0)\n\t"
        "global_load_dword %[ep], %[vz], %[pws] sc1\n"
        "1:\n\ts_cmp_gt_u32 %[w], 3\n\ts_cbranch_scc1 2f\n\t"
        "s_waitcnt lgkmcnt(0)\n\t"
        "global_load_dwordx4 %[b0], %[o0], %[pb] nt\n\t"
        "global_load_dwordx4 %[b1], %[o1], %[pb] nt\n\t"
        "global_load_dwordx4 %[b2], %[o2], %[pb] nt\n\t"
        "global_load_dwordx4 %[b3], %[o3], %[pb] nt\n\t"
        "global_load_dwordx2 %[xa], %[xoff], %[px]\n\t"
        "global_load_dwordx2 %[xb], %[xoff], %[px] offset:1024\n"
        "2:"
        : [cw] "=&v"(codew), [ep] "=&v"(epoch), [xa] "=&v"(xq0), [xb] "=&v"(xq1), [b0] "=&v"(big[0]), [b1] "=&v"(big[1]),
          [b2] "=&v"(big[2]), [b3] "=&v"(big[3])
        : [w] "s"(w), [coff] "v"(coff), [pc] "s"(pc0 & ~127ull), [xoff] "v"(qrow * 2), [px] "s"(p_x), [vz] "v"(0), [pws] "s"(p_ws),
          [pb] "s"(p_wq), [o0] "v"(qo), [o1] "v"(qo + HD * 2), [o2] "v"(qo + HD * 4), [o3] "v"(qo + HD * 6)
        : "memory", "scc");
  }
#define WGBAR_A()                                                   \
  do {                                                              \
    WGBAR();                                                        \
    asm volatile("" ::"s"(p_x), "s"(p_wq), "s"(p_ws) STAMP_KEEP);   \
  } while (0)
#define PRO_WAIT(N)                                             \
  do {                                                          \
    SB();                                                       \
    asm volatile("s_waitcnt vmcnt(" #N ")" ::: "memory");       \
    SB();                                                       \
  } while (0)
  const int sw = (r & 7) * 9;
  const size_t kvbase = ((size_t)((r >> 4) * 8 + grp) * SK + (SK - WINDOW) + (r & 15) * 256) * DH;
  const int j = lane >> 4, n = lane & 15;
#define MAKE_RS() asm volatile("" : "+s"(ws)); const __amdgpu_buffer_rsrc_t rs = __builtin_amdgcn_make_buffer_rsrc(ws, 0, WS_END, 0x00020000)
  const f32x4 zero4 = {0.f, 0.f, 0.f, 0.f};
  constexpr float LOG2E = 1.4426950408889634f;

  if (w < 4) {
    WGBAR_A();
    PRO_WAIT(0);
    asm volatile("" : "+v"(xq0), "+v"(xq1), "+v"(big[0]), "+v"(big[1]), "+v"(big[2]), "+v"(big[3]));
    {
      float v[16];
#pragma unroll
      for (int e = 0; e < 8; ++e) {
        const unsigned p01 = permsel(big[1][e >> 1], big[0][e >> 1], e), p23 = permsel(big[3][e >> 1], big[2][e >> 1], e);
        v[e] = dot2(p23, xq0[1], dot2(p01, xq0[0], 0.f));
        v[8 + e] = dot2(p23, xq1[1], dot2(p01, xq1[0], 0.f));
      }
      const f32x4 a = reduce16(v);
      if ((lane & 8) == 0) *(LAS f32x4*)(L + L_QRED + ((w * 2 + (lane >> 5)) * 64 + 8 * (lane & 7) + 4 * ((lane >> 4) & 1)) * 4) = a;
    }
    lds_signal(cnt + 6);
    STAMP(1);
    MAKE_RS();
    lds_wait(cnt + 7, 2);
    v4u vf[16];
#pragma unroll
    for (int i = 0; i < 16; ++i) vf[i] = ldnt(Vc + kvbase + ((16 * w + i) ^ sw) * 512 + lane * 8);
    SB();
    lds_signal(cnt + 9);
    STAMP(2);
    v4u be[8];
#pragma unroll
    for (int q = 0; q < 4; ++q) {
#pragma unroll
      for (int e = 0; e < 8; ++e) be[e][q] = permsel(vf[2 * q + 1][e >> 1], vf[2 * q][e >> 1], e);
      SB();
    }
#pragma unroll
    for (int e = 0; e < 8; ++e) asm volatile("" : "+v"(be[e]));
    lds_signal(cnt + 0);
    STAMP(3);
    lds_wait(cnt + 5, 4);
    STAMP(4);
    __builtin_amdgcn_s_setprio(3);
    const v4u pa0 = *(LAS v4u*)(L + L_P + (w * 2) * 1024 + lane * 16), pa1 = *(LAS v4u*)(L + L_P + (w * 2 + 1) * 1024 + lane * 16);
    const float Ls = sum4(*(LAS f32x4*)(L + L_LSUM + w * 16));
    const float Mh = max4(*(LAS f32x4*)(L + L_WMAX + w * 16));
    const unsigned tag = cnt[12];
    f32x4 oacc[8];
#pragma unroll
    for (int e = 0; e < 8; ++e) oacc[e] = mfma32(pa0, be[e], zero4);
    float inv = __builtin_amdgcn_rcpf(Ls);
    const unsigned lse = __float_as_uint(fmaf(Mh, LOG2E, __builtin_amdgcn_logf(Ls)));
    unsigned lA = lse ^ tag, lB = lse ^ tag_b(tag);
    int s2dst = lane < 32 ? S2_OFF + (grp * 32 + r) * 2048 + w * 512 + lane * 16 : OOB;
    int src = L_OP + (lane >> 5) * 2 * OP_W + (lane & 1) * 1152 + (w * 16 + ((lane & 31) >> 1)) * 16;
    asm volatile("" : "+v"(inv), "+v"(lA), "+v"(lB), "+v"(s2dst), "+v"(src));
    SB();
    STAMP(5);
#pragma unroll
    for (int q = 0; q < 4; ++q) {
#pragma unroll
      for (int e = 0; e < 8; ++e) be[e][q] = permsel(vf[8 + 2 * q + 1][e >> 1], vf[8 + 2 * q][e >> 1], e);
      if (q < 3) SB();
    }
#pragma unroll
    for (int e = 0; e < 8; ++e) oacc[e] = mfma32(pa1, be[e], oacc[e]);
    *(LAS f32x4*)(L + L_OP + w * OP_W + lane * 16) = f32x4{oacc[0][0], oacc[1][0], oacc[2][0], oacc[3][0]};
    *(LAS f32x4*)(L + L_OP + w * OP_W + 1152 + lane * 16) = f32x4{oacc[4][0], oacc[5][0], oacc[6][0], oacc[7][0]};
    STAMP(7);
    WGBAR();
    {
      const f32x4 t = *(LAS f32x4*)(L + src) + *(LAS f32x4*)(L + src + OP_W);
      const float a0 = swapsum32(t[0], t[0]), a1 = swapsum32(t[1], t[1]), a2 = swapsum32(t[2], t[2]), a3 = swapsum32(t[3], t[3]);
      st_sc1(rs, s2dst, v4u{cvtpk(a0 * inv, a1 * inv), lA, cvtpk(a2 * inv, a3 * inv), lB});
    }
    STAMP(8);
    STAMP_FLUSH(9);
    return;
  }

  if (w < 8) {
    __builtin_amdgcn_s_setprio(2);
    const int k = w - 4;
    const u16* kp = Kc + kvbase + lane * 8;
    WGBAR_A();
    v4u kf[16];
#pragma unroll
    for (int i = 0; i < GM_K0; ++i) kf[i] = ldnt(kp + ((16 * k + i) ^ sw) * 512);
    SB();
    STAMP(1);
    lds_wait(cnt + 8, 1);
#pragma unroll
    for (int i = GM_K0; i < 16; ++i) kf[i] = ldnt(kp + ((16 * k + i) ^ sw) * 512);
    SB();
    STAMP(2);
    LAS unsigned char* Kl = L + L_K + k * 16384;
#pragma unroll
    for (int i = 0; i < 16; ++i) {
      const int key = 4 * i + j;
      *(LAS v4u*)(Kl + key * 256 + ((n ^ (key & 15)) << 4)) = kf[i];
    }
    v4u ka[4][4];
    {
      const int kin = 4 * (n & 3) + (n >> 2);
#pragma unroll
      for (int t = 0; t < 4; ++t)
#pragma unroll
        for (int st = 0; st < 4; ++st) ka[t][st] = *(LAS v4u*)(Kl + (16 * t + kin) * 256 + (((4 * st + j) ^ kin) << 4));
    }
    STAMP(3);
    lds_wait(cnt + 2, 2);
    STAMP(4);
    v4u qf[4];
#pragma unroll
    for (int st = 0; st < 4; ++st) qf[st] = *(LAS v4u*)(L + L_Q + (n >> 2) * 256 + (4 * st + j) * 16);
    f32x4 sc[4];
#pragma unroll
    for (int t = 0; t < 4; ++t) sc[t] = zero4;
#pragma unroll
    for (int st = 0; st < 4; ++st)
#pragma unroll
      for (int t = 0; t < 4; ++t) sc[t] = mfma32(ka[t][st], qf[st], sc[t]);
#pragma unroll
    for (int t = 0; t < 4; ++t) {
      const unsigned a = cvtpk(sc[t][0], sc[t][1]), b = cvtpk(sc[t][2], sc[t][3]);
      const unsigned c = cvtpk(bflo(a) * 0.08838834764831845f, bfhi(a) * 0.08838834764831845f);
      const unsigned d = cvtpk(bflo(b) * 0.08838834764831845f, bfhi(b) * 0.08838834764831845f);
      sc[t] = f32x4{bflo(c), bfhi(c), bflo(d), bfhi(d)};
    }
    const float mx = xrow_max(fmaxf(fmaxf(max4(sc[0]), max4(sc[1])), fmaxf(max4(sc[2]), max4(sc[3]))));
    if (lane < 16 && (lane & 3) == 0) *(LAS float*)(L + L_WMAX + (lane + k) * 4) = mx;
    lds_signal(cnt + 1);
    lds_wait(cnt + 1, 4);
    STAMP(5);
    const float Mc = LOG2E * max4(*(LAS f32x4*)(L + L_WMAX + (n >> 2) * 16));
    v4u pa[2];
#pragma unroll
    for (int t = 0; t < 4; ++t) {
      float p[4];
#pragma unroll
      for (int i = 0; i < 4; ++i) p[i] = __builtin_amdgcn_exp2f(fmaf(sc[t][i], LOG2E, -Mc));
      pa[t >> 1][2 * (t & 1)] = cvtpk(p[0], p[1]);
      pa[t >> 1][2 * (t & 1) + 1] = cvtpk(p[2], p[3]);
    }
    *(LAS v4u*)(L + L_P + (k * 2) * 1024 + lane * 16) = pa[0];
    *(LAS v4u*)(L + L_P + (k * 2 + 1) * 1024 + lane * 16) = pa[1];
    const v4u ones = {0x3F803F80u, 0x3F803F80u, 0x3F803F80u, 0x3F803F80u};
    const f32x4 ls = mfma32(pa[1], ones, mfma32(pa[0], ones, zero4));
    if (n == 0) *(LAS float*)(L + L_LSUM + (j * 4 + k) * 4) = ls[0];
    lds_signal(cnt + 5);
    STAMP(6);
    STAMP_FLUSH(9);
    return;
  }

  {
    __builtin_amdgcn_s_setprio(3);
    const int tw = w - 8;
    MAKE_RS();
    const int cblk = r & 7, hrel = r >> 3;
    const u16* wop = wo + (size_t)(512 * grp + 128 * hrel + 32 * tw + 4 * (lane >> 3)) * DM + 64 * cblk + 8 * (lane & 7);
    const u16* wdp = wd + (size_t)(224 * grp + 56 * hrel + 14 * tw + (lane < 56 ? 2 * (lane >> 3) : 12)) * DM + 64 * cblk + 8 * (lane & 7);
    int s2off = S2_OFF + (grp * 32 + (lane >> 3)) * 2048 + hrel * 512 + tw * 128 + (lane & 7) * 16;
    int s3off = S3_OFF + ((bid >> 2) * 32 + (lane >> 3)) * 512 + (bid & 3) * 128 + (lane & 7) * 16;
    asm volatile("" : "+v"(s2off), "+v"(s3off));
    v4u wfr[4], dfr[2];
#define ISSUE_WO_WD()                                                             \
  do {                                                                            \
    lds_wait(cnt + (GM_WSIG ? 0 : 9), 4);                                         \
    STAMP(5);                                                                     \
    _Pragma("unroll") for (int i = 0; i < 4; ++i) wfr[i] = ldnt(wop + i * DM);    \
    dfr[0] = ldnt(wdp);                                                           \
    dfr[1] = ldnt(wdp + DM);                                                      \
    SB();                                                                         \
  } while (0)
#define TAGS_LDS()                                                                \
  do {                                                                            \
    tag = cnt[12];                                                                \
    tagB = tag_b(tag);                                                            \
    tgx = tag ^ tagB;                                                             \
  } while (0)
#define TAGS()                                                                    \
  do {                                                                            \
    asm volatile("" : "+v"(epoch));                                               \
    unsigned tg = epoch * 1664525u + 1013904223u;   \
    if (tg == 0u) tg = 1013904223u;                 \
    tag = tg;                                                                     \
    tagB = tag_b(tg);                                                             \
    tgx = tag ^ tagB;                                                             \
    asm volatile("" : "+v"(tag), "+v"(tagB), "+v"(tgx));                          \
  } while (0)
    unsigned tag, tagB, tgx;
    bool alive = true;
    const int ft = bid < 224 ? bid : bid - 32;
    const int fmat = ft >= 112, fcb = (ft - 112 * fmat) >> 2, frb = ft & 3;
    if (tw < 2) {
      if (tw == 1) {
        if (lane < 12) cnt[lane] = 0u;
        WGBAR_A();
        PRO_WAIT(0);
        asm volatile("" ::"v"(codew));
        TAGS();
        if (lane == 0) cnt[12] = tag;
        STAMP(1);
        lds_wait(cnt + 6, 4);
        f32x4 a = *(LAS f32x4*)(L + L_QRED + (lane >> 5) * 1024 + (lane & 31) * 16) + *(LAS f32x4*)(L + L_QRED + (lane >> 5) * 1024 + 512 + (lane & 31) * 16);
        a = f32x4{swapsum32(a[0], a[0]), swapsum32(a[1], a[1]), swapsum32(a[2], a[2]), swapsum32(a[3], a[3])};
        st_sc1(rs, lane < 32 ? QG_OFF + ((grp * 8 + (r & 7)) * 4 + (r >> 3)) * 512 + lane * 16 : OOB, pack4(a, tag, tagB));
        lds_signal(cnt + 8);
        STAMP(2);
      } else {
        const v4u gv = *(const GAS v4u*)(gw + 8 * lane);
        const v4u xa = *(const GAS v4u*)(x + 8 * lane), xb = *(const GAS v4u*)(x + DM + 8 * lane);
        WGBAR_A();
        float a0 = 0.f, a1 = 0.f;
#pragma unroll
        for (int q = 0; q < 4; ++q) {
          a0 += bfround(bflo(gv[q]) * bflo(xa[q])) + bfround(bfhi(gv[q]) * bfhi(xa[q]));
          a1 += bfround(bflo(gv[q]) * bflo(xb[q])) + bfround(bfhi(gv[q]) * bfhi(xb[q]));
        }
        float a = swapsum32(a0, a1);
        a = swapsum16(a, a);
        a += dpp<D_ROR8>(a);
        a += dpp<D_ROR4>(a);
        a += dpp<D_XOR2>(a);
        a += dpp<D_XOR1>(a);
        const float gt = bfround(__builtin_amdgcn_rcpf(1.0f + __expf(-bfround(a))));
        if ((lane & 31) == 0) cnt[13 + (lane >> 5)] = __float_as_uint(gt);
        STAMP(1);
        lds_wait(cnt + 8, 1);
        TAGS_LDS();
      }
      const int qoff = QG_OFF + (grp * 8 + 4 * tw + (lane >> 4)) * 2048 + (r >> 4) * 256 + (lane & 15) * 16;
      v4u pq[2][4];
#pragma unroll
      for (int kk = 0; kk < 2; ++kk) {
        if (kk == 0) __builtin_amdgcn_s_sleep(GM_PS0);
        else __builtin_amdgcn_s_sleep(GM_PS1);
        CBAR();
#pragma unroll
        for (int p = 0; p < 4; ++p) pq[kk][p] = ld_sc1(rs, qoff + p * 512);
        SB();
        if (kk == 0) lds_signal(cnt + 7);
      }
      STAMP(3);
      f32x4 qv;
      if (__all(BAD4(pq[0]) == 0u)) {
        qv = USUM4(pq[0]);
      } else {
        bool done = __all(BAD4(pq[1]) == 0u);
        unsigned spins = 0;
#pragma nounroll
        while (!done && ++spins < SPIN_CAP) {
          CBAR();
#pragma unroll
          for (int p = 0; p < 4; ++p) pq[1][p] = ld_sc1(rs, qoff + p * 512);
          done = __all(BAD4(pq[1]) == 0u);
        }
        alive = done;
        qv = USUM4(pq[1]);
      }
      *(LAS v2u*)(L + L_Q + tw * 512 + lane * 8) = v2u{cvtpk(qv[0], qv[1]), cvtpk(qv[2], qv[3])};
      lds_signal(cnt + 2);
      STAMP(4);
      asm volatile("" ::"v"(pq[1][0]), "v"(pq[1][1]), "v"(pq[1][2]), "v"(pq[1][3]));
      ISSUE_WO_WD();
      STAMP(6);
      WGBAR();
      STAMP(7);
    } else {
      const int frow = 128 * frb + 64 * (tw & 1) + 8 * (lane >> 3);
      const v4u xa = *(const GAS v4u*)(x + frow), xb = *(const GAS v4u*)(x + DM + frow);
      const u16* wfp = (fmat ? wu : wg) + (size_t)frow * FF + 64 * fcb + 8 * (lane & 7);
      WGBAR_A();
      v4u wf[8];
#pragma unroll
      for (int i = 0; i < 8; ++i) wf[i] = ldnt(wfp + i * FF);
      SB();
      float v[16];
#pragma unroll
      for (int e = 0; e < 8; ++e) {
        v[e] = 0.f; v[8 + e] = 0.f;
#pragma unroll
        for (int q = 0; q < 4; ++q) {
          const unsigned pk = permsel(wf[2 * q + 1][e >> 1], wf[2 * q][e >> 1], e);
          v[e] = dot2(pk, xa[q], v[e]);
          v[8 + e] = dot2(pk, xb[q], v[8 + e]);
        }
      }
      lds_wait(cnt + 8, 1);
      TAGS_LDS();
      const f32x4 fa = reduce16(v);
      if ((lane & 8) == 0) *(LAS f32x4*)(L + L_FRED + (((tw & 1) * 2 + (lane >> 5)) * 64 + 8 * (lane & 7) + 4 * ((lane >> 4) & 1)) * 4) = fa;
      if (lds_signal_ret(cnt + 4) == 1u && bid < 224 && lane < 32) {
        const f32x2 a0 = *(LAS f32x2*)(L + L_FRED + lane * 8) + *(LAS f32x2*)(L + L_FRED + 512 + lane * 8);
        const f32x2 a1 = *(LAS f32x2*)(L + L_FRED + 256 + lane * 8) + *(LAS f32x2*)(L + L_FRED + 768 + lane * 8);
        st_sc1(rs, F1_OFF + ft * 512 + lane * 16, pack4(f32x4{a0[0], a0[1], a1[0], a1[1]}, tag, tagB));
      }
      STAMP(3);
      const int cc = 224 * grp + 56 * hrel + 2 * ((lane & 31) < 28 ? (lane & 31) : 27);
      const int hoff = F1_OFF + ((lane >> 5) * 28 + (cc >> 6)) * 2048 + (cc & 63) * 8;
      lds_wait(cnt + 9, 4);
      v4u hg[4], t0, t1;
#pragma unroll
      for (int p = 0; p < 4; ++p) hg[p] = ld_sc1(rs, tw == 2 ? hoff + p * 512 : bid < 32 ? s3off + p * 4096 : OOB);
      t0 = ld_sc1(rs, tw == 3 ? S2_OFF + (grp * 32 + r) * 2048 + lane * 16 : OOB);
      t1 = ld_sc1(rs, tw == 3 ? S2_OFF + (grp * 32 + r) * 2048 + 1024 + lane * 16 : OOB);
      SB();
      STAMP(4);
      ISSUE_WO_WD();
      STAMP(6);
      WGBAR();
      STAMP(7);
      asm volatile("" ::"v"(t0), "v"(t1));
      if (tw == 2) {
        if (!__all(BAD4(hg) == 0u)) {
          unsigned spins = 0;
          bool done;
#pragma nounroll
          do {
            CBAR();
#pragma unroll
            for (int p = 0; p < 4; ++p) hg[p] = ld_sc1(rs, hoff + p * 512);
            done = __all(BAD4(hg) == 0u);
          } while (!done && ++spins < SPIN_CAP);
          alive = done;
        }
        const f32x4 hsum = USUM4(hg);
        float ga[2], ua[2];
#pragma unroll
        for (int c = 0; c < 2; ++c) {
          auto sres = __builtin_amdgcn_permlane32_swap(__float_as_uint(hsum[c]), __float_as_uint(hsum[2 + c]), false, false);
          ga[c] = __uint_as_float(sres[0]);
          ua[c] = __uint_as_float(sres[1]);
        }
        const float omg = bfround(1.0f - __uint_as_float(cnt[13 + (lane >> 5)]));
        float hv2[2];
#pragma unroll
        for (int c = 0; c < 2; ++c) {
          const float gb = bfround(ga[c]);
          const float si = bfround(gb * __builtin_amdgcn_rcpf(1.0f + __expf(-gb)));
          hv2[c] = bfround(si * bfround(ua[c])) * omg;
        }
        unsigned hp = cvtpk(hv2[0], hv2[1]);
        if (!alive) hp = 0x7FC07FC0u;
        *(LAS unsigned*)(L + L_H + lane * 4) = hp;
        lds_signal(cnt + 3);
      } else {
        asm volatile("" ::"v"(hg[0]), "v"(hg[1]), "v"(hg[2]), "v"(hg[3]));
      }
    }
    const float gate = __uint_as_float(cnt[13 + (lane >> 5)]);
    unsigned o0[4], o1[4], o2[4];
    v4u wb[8];
    {
      bool ho[4];
      v4u od[4];
      __builtin_amdgcn_s_sleep(GM_S2_DELAY);
#pragma unroll
      for (int i = 0; i < 4; ++i) od[i] = ld_sc1(rs, s2off + i * 8 * 2048);
      SB();
#pragma unroll
      for (int e = 0; e < 8; ++e) {
        wb[e][0] = permsel(wfr[1][e >> 1], wfr[0][e >> 1], e);
        wb[e][1] = permsel(wfr[3][e >> 1], wfr[2][e >> 1], e);
        wb[e][2] = lane < 56 ? permsel(dfr[1][e >> 1], dfr[0][e >> 1], e) : 0u;
        wb[e][3] = 0u;
        asm volatile("" : "+v"(wb[e]));
      }
      SB();
      STAMP(15);
#pragma unroll
      for (int i = 0; i < 4; ++i) {
        ho[i] = (od[i][1] ^ od[i][3]) == tgx;
        o0[i] = od[i][0]; o1[i] = od[i][1]; o2[i] = od[i][2];
      }
      unsigned spins = 0;
#pragma nounroll
      while (true) {
        const int miss = __builtin_popcountll(__ballot(!ho[0])) + __builtin_popcountll(__ballot(!ho[1])) +
                         __builtin_popcountll(__ballot(!ho[2])) + __builtin_popcountll(__ballot(!ho[3]));
        if (miss == 0) break;
        if (++spins >= SPIN_CAP) { alive = false; break; }
        if (miss > 128) __builtin_amdgcn_s_sleep(GM_SL_HI);
        else if (miss > 40) __builtin_amdgcn_s_sleep(GM_SL_MID);
        CBAR();
#pragma unroll
        for (int i = 0; i < 4; ++i) od[i] = ld_sc1(rs, ho[i] ? OOB : s2off + i * 8 * 2048);
        SB();
#pragma unroll
        for (int i = 0; i < 4; ++i) {
          o0[i] = ho[i] ? o0[i] : od[i][0];
          o1[i] = ho[i] ? o1[i] : od[i][1];
          o2[i] = ho[i] ? o2[i] : od[i][2];
          ho[i] = ho[i] | ((od[i][1] ^ od[i][3]) == tgx);
        }
      }
    }
    STAMP(9);
    {
      const int rho = 2 * j + ((n >> 2) & 1);
      float ls[4], ov[10];
#pragma unroll
      for (int i = 0; i < 4; ++i) ls[i] = __uint_as_float(o1[i] ^ tag);
#pragma unroll
      for (int bb = 0; bb < 2; ++bb) {
        float mx = fmaxf(ls[2 * bb], ls[2 * bb + 1]);
        mx = fmaxf(mx, dpp<D_ROR8>(mx));
        mx = xrow_max(mx);
        const float w0 = __builtin_amdgcn_exp2f(ls[2 * bb] - mx), w1 = __builtin_amdgcn_exp2f(ls[2 * bb + 1] - mx);
        ov[4 * bb + 0] = w0 * bflo(o0[2 * bb]) + w1 * bflo(o0[2 * bb + 1]);
        ov[4 * bb + 1] = w0 * bfhi(o0[2 * bb]) + w1 * bfhi(o0[2 * bb + 1]);
        ov[4 * bb + 2] = w0 * bflo(o2[2 * bb]) + w1 * bflo(o2[2 * bb + 1]);
        ov[4 * bb + 3] = w0 * bfhi(o2[2 * bb]) + w1 * bfhi(o2[2 * bb + 1]);
        ov[8 + bb] = w0 + w1;
      }
#pragma unroll
      for (int i = 0; i < 4; ++i) ov[i] = swapsum32(ov[i], ov[i + 4]);
      float W = swapsum32(ov[8], ov[9]);
      ov[0] = swapsum16(ov[0], ov[2]);
      ov[1] = swapsum16(ov[1], ov[3]);
      W = swapsum16(W, W);
      ov[0] += dpp<D_ROR8>(ov[0]);
      ov[1] += dpp<D_ROR8>(ov[1]);
      W += dpp<D_ROR8>(W);
      const float sc = gate * __builtin_amdgcn_rcpf(W);
      *(LAS unsigned*)(L + L_OA + tw * 128 + (lane >> 5) * 64 + (lane & 7) * 8 + ((lane >> 4) & 1) * 4) = cvtpk(sc * ov[0], sc * ov[1]);
      lds_wait(cnt + 3, 1);
      const v2u ao = *(LAS v2u*)(L + L_OA + tw * 128 + (n >> 3) * 64 + rho * 8);
      const unsigned ah = *(LAS unsigned*)(L + L_H + (n >> 3) * 128 + tw * 28 + rho * 4);
      const v4u am = {ao[0], ao[1], rho < 7 ? ah : 0u, 0u};
      float res[8];
#pragma unroll
      for (int e = 0; e < 8; ++e) res[e] = mfma32(am, wb[e], zero4)[0];
      if (!alive) res[0] = __uint_as_float(0x7FC00000u);
      if ((j & 1) == (n >> 3)) {
        LAS unsigned char* dst = L + L_RED + ((tw * 2 + (j & 1)) * 2 + (j >> 1)) * 256 + (n & 7) * 32;
        *(LAS f32x4*)dst = f32x4{res[0], res[1], res[2], res[3]};
        *(LAS f32x4*)(dst + 16) = f32x4{res[4], res[5], res[6], res[7]};
      }
    }
    STAMP(10);
    WGBAR();
    if (tw == 0) {
      f32x4 a = zero4;
#pragma unroll
      for (int t = 0; t < 4; ++t) a += *(LAS f32x4*)(L + L_RED + ((lane >> 5) * 4 + t) * 512 + (lane & 31) * 16);
      const f32x4 s = {swapsum32(a[0], a[0]), swapsum32(a[1], a[1]), swapsum32(a[2], a[2]), swapsum32(a[3], a[3])};
      if (lane < 32) st_sc1(rs, S3_OFF + (cblk * 32 + 4 * grp + hrel) * 512 + lane * 16, pack4(s, tag, tagB));
      STAMP(11);
    } else if (tw == 1 && bid < 32) {
      v4u pd[4];
      unsigned spins = 0;
      bool done;
#pragma nounroll
      do {
        CBAR();
#pragma unroll
        for (int i = 0; i < 4; ++i) pd[i] = ld_sc1(rs, s3off + i * 4096);
        SB();
        done = __all(BAD4(pd) == 0u);
      } while (!done && ++spins < SPIN_CAP);
      alive = alive && done;
      STAMP(12);
      const f32x4 a = USUM4(pd);
      float b0 = swapsum32(a[0], a[2]), b1 = swapsum32(a[1], a[3]);
      b0 = swapsum16(b0, b0);
      b1 = swapsum16(b1, b1);
      b0 += dpp<D_ROR8>(b0);
      b1 += dpp<D_ROR8>(b1);
      unsigned pk = cvtpk(b0, b1);
      if (!alive) pk = 0x7FC07FC0u;
      if ((lane & 24) == 0)
        *(GAS unsigned*)(out + ((bid >> 1) & 1) * DM + 64 * (bid >> 2) + 32 * (bid & 1) + 4 * (lane & 7) + 2 * (lane >> 5)) = pk;
      if (bid == 0 && lane == 0) __hip_atomic_store((GAS unsigned*)(ws + EPOCH_OFF), tag, RLX_AGENT);
      STAMP(13);
    }
    STAMP_FLUSH(14);
  }
}

}

extern "C" void kernel_launch(void* const* d_in, const int* in_sizes, int n_in, void* d_out, int out_size, void* d_ws,
                              size_t ws_size, hipStream_t stream) {
  using gm::u16;
  (void)in_sizes; (void)n_in; (void)out_size; (void)ws_size;
  gm::fused_kernel<<<gm::NWG, gm::NTHR, 0, stream>>>(
      (const u16*)d_in[0], (const u16*)d_in[3], (unsigned char*)d_ws, (const u16*)d_in[9], (const u16*)d_in[1],
      (const u16*)d_in[6], (const u16*)d_in[7], (const u16*)d_in[2], (const u16*)d_in[4], (const u16*)d_in[8], (u16*)d_out);
}
```

```cpp
#include <hip/hip_runtime.h>
#include <hip/hip_bf16.h>
#include <stdint.h>

namespace gm {

typedef unsigned v4u __attribute__((ext_vector_type(4)));
typedef unsigned v2u __attribute__((ext_vector_type(2)));
typedef float f32x4 __attribute__((ext_vector_type(4)));
typedef float f32x2 __attribute__((ext_vector_type(2)));
typedef __bf16 bf16x8_t __attribute__((ext_vector_type(8)));
typedef __bf16 bf16x2_t __attribute__((ext_vector_type(2)));
typedef unsigned short u16;

#define GAS __attribute__((address_space(1)))
#define LAS __attribute__((address_space(3)))
#define RLX_AGENT __ATOMIC_RELAXED, __HIP_MEMORY_SCOPE_AGENT

constexpr int SK = 32768, WINDOW = 4096, DH = 128, DM = 512, FF = 1792, HD = 4096;
constexpr int NWG = 256, NTHR = 768, NWAVES = 12;

constexpr int EPOCH_OFF = 0;
constexpr int QG_OFF = 4096;
constexpr int F1_OFF = QG_OFF + 256 * 512;
constexpr int S2_OFF = F1_OFF + 224 * 512;
constexpr int S3_OFF = S2_OFF + 8 * 32 * 2048;
constexpr int WS_END = S3_OFF + 8 * 32 * 512;
constexpr int STAMP_OFF = 4 << 20;

constexpr int L_CNT = 0;
constexpr int L_QRED = 64;
constexpr int L_FRED = L_QRED + 2048;
constexpr int L_Q = L_FRED + 1024;
constexpr int L_H = L_Q + 1024;
constexpr int L_OA = L_H + 256;
constexpr int L_WMAX = L_OA + 512;
constexpr int L_LSUM = L_WMAX + 64;
constexpr int L_RED = L_LSUM + 64;
constexpr int L_P = L_RED + 4096;
constexpr int L_OP = L_P + 8192;
constexpr int OP_W = 2304;
constexpr int L_K = L_OP + 4 * OP_W;
constexpr int L_DUMP = L_K;
constexpr int L_END = L_K + 4 * 16384;
constexpr int LDS_BYTES = 100 * 1024;
static_assert(L_END + 512 + 1536 <= LDS_BYTES && (L_OP % 16) == 0 && (L_K % 16) == 0 && (L_RED % 16) == 0 && (L_P % 16) == 0, "lds map");

constexpr unsigned SPIN_CAP = 1u << 18;
constexpr int AUX_SC1 = 16;
constexpr int OOB = 0x7FFFFF00;

__device__ __forceinline__ v4u ldnt(const void* p) { return __builtin_nontemporal_load((const GAS v4u*)p); }
__device__ __forceinline__ float dot2(unsigned a, unsigned b, float c) {
  return __builtin_amdgcn_fdot2_f32_bf16(__builtin_bit_cast(bf16x2_t, a), __builtin_bit_cast(bf16x2_t, b), c, false);
}
__device__ __forceinline__ unsigned cvtpk(float lo, float hi) {
  bf16x2_t r = {(__bf16)lo, (__bf16)hi};
  return __builtin_bit_cast(unsigned, r);
}
__device__ __forceinline__ float bfround(float v) { return (float)(__bf16)v; }
__device__ __forceinline__ float bflo(unsigned u) { return __uint_as_float(u << 16); }
__device__ __forceinline__ float bfhi(unsigned u) { return __uint_as_float(u & 0xffff0000u); }
template <int CTRL> __device__ __forceinline__ float dpp(float x) {
  return __builtin_bit_cast(float, __builtin_amdgcn_mov_dpp(__builtin_bit_cast(int, x), CTRL, 0xf, 0xf, true));
}
constexpr int D_XOR1 = 0xB1, D_XOR2 = 0x4E, D_ROR8 = 0x128, D_ROR4 = 0x124;
__device__ __forceinline__ float swapsum32(float a, float b) {
  auto s = __builtin_amdgcn_permlane32_swap(__float_as_uint(a), __float_as_uint(b), false, false);
  return __uint_as_float(s[0]) + __uint_as_float(s[1]);
}
__device__ __forceinline__ float swapsum16(float a, float b) {
  auto s = __builtin_amdgcn_permlane16_swap(__float_as_uint(a), __float_as_uint(b), false, false);
  return __uint_as_float(s[0]) + __uint_as_float(s[1]);
}
__device__ __forceinline__ float xrow_max(float x) {
  auto s = __builtin_amdgcn_permlane16_swap(__float_as_uint(x), __float_as_uint(x), false, false);
  x = fmaxf(__uint_as_float(s[0]), __uint_as_float(s[1]));
  auto t = __builtin_amdgcn_permlane32_swap(__float_as_uint(x), __float_as_uint(x), false, false);
  return fmaxf(__uint_as_float(t[0]), __uint_as_float(t[1]));
}
__device__ __forceinline__ float max4(f32x4 a) { return fmaxf(fmaxf(a[0], a[1]), fmaxf(a[2], a[3])); }
__device__ __forceinline__ float sum4(f32x4 a) { return (a[0] + a[1]) + (a[2] + a[3]); }
__device__ __forceinline__ f32x4 mfma32(v4u a, v4u b, f32x4 c) {
  return __builtin_amdgcn_mfma_f32_16x16x32_bf16(__builtin_bit_cast(bf16x8_t, a), __builtin_bit_cast(bf16x8_t, b), c, 0, 0, 0);
}
__device__ __forceinline__ unsigned permsel(unsigned hi, unsigned lo, int e) {
  return __builtin_amdgcn_perm(hi, lo, (e & 1) ? 0x07060302u : 0x05040100u);
}
__device__ __forceinline__ v4u ld_sc1(__amdgpu_buffer_rsrc_t rs, int off) {
  return __builtin_amdgcn_raw_buffer_load_b128(rs, off, 0, AUX_SC1);
}
__device__ __forceinline__ void st_sc1(__amdgpu_buffer_rsrc_t rs, int off, v4u v) {
  __builtin_amdgcn_raw_buffer_store_b128(v, rs, off, 0, AUX_SC1);
}
__device__ __forceinline__ unsigned tag_b(unsigned t) { return (t << 1) ^ ((t >> 31) ? 0x04C11DB7u : 0u); }
__device__ __forceinline__ v4u pack4(f32x4 v, unsigned tA, unsigned tB) {
  const unsigned u3 = __float_as_uint(v[3]);
  const unsigned a = __builtin_amdgcn_perm(__float_as_uint(v[0]), u3, 0x07060503u);
  const unsigned b = __builtin_amdgcn_perm(__float_as_uint(v[1]), u3, 0x07060502u);
  const unsigned c = __builtin_amdgcn_perm(__float_as_uint(v[2]), u3, 0x07060501u);
  return v4u{a, c ^ tA, b, c ^ tB};
}
__device__ __forceinline__ f32x4 unpack4(v4u p, unsigned tA) {
  const unsigned c = p[1] ^ tA;
  const unsigned t = __builtin_amdgcn_perm(p[0], p[2], 0x04000c0cu);
  const unsigned u3 = __builtin_amdgcn_perm(t, c, 0x0706000cu);
  return f32x4{__uint_as_float(p[0] & 0xffffff00u), __uint_as_float(p[2] & 0xffffff00u), __uint_as_float(c & 0xffffff00u), __uint_as_float(u3)};
}
#define SB() __builtin_amdgcn_sched_barrier(0)
#define WGBAR()                                                     \
  do {                                                              \
    SB();                                                           \
    asm volatile("s_waitcnt lgkmcnt(0)\n\ts_barrier" ::: "memory"); \
    SB();                                                           \
  } while (0)
#define CBAR() asm volatile("" ::: "memory")
__device__ __forceinline__ void lds_signal(LAS unsigned* p) {
  asm volatile("s_waitcnt lgkmcnt(0)" ::: "memory");
  if ((threadIdx.x & 63) == 0) __hip_atomic_fetch_add(p, 1u, __ATOMIC_RELAXED, __HIP_MEMORY_SCOPE_WORKGROUP);
  CBAR();
}
__device__ __forceinline__ unsigned lds_signal_ret(LAS unsigned* p) {
  asm volatile("s_waitcnt lgkmcnt(0)" ::: "memory");
  unsigned old = 0;
  if ((threadIdx.x & 63) == 0) old = __hip_atomic_fetch_add(p, 1u, __ATOMIC_RELAXED, __HIP_MEMORY_SCOPE_WORKGROUP);
  old = __builtin_amdgcn_readfirstlane(old);
  CBAR();
  return old;
}
__device__ __forceinline__ void lds_wait(LAS unsigned* p, unsigned want) {
  SB();
  while (__builtin_amdgcn_readfirstlane(*(volatile LAS unsigned*)p) < want) __builtin_amdgcn_s_sleep(1);
  CBAR();
  SB();
}
__device__ __forceinline__ void lds_wait_fast(LAS unsigned* p, unsigned want) {
  SB();
  while (__builtin_amdgcn_readfirstlane(*(volatile LAS unsigned*)p) < want) {}
  CBAR();
  SB();
}
__device__ __forceinline__ void wait_until(unsigned long long t) {
  while (__builtin_amdgcn_s_memrealtime() < t) __builtin_amdgcn_s_sleep(1);
}
__device__ __forceinline__ f32x4 reduce16(float (&v)[16]) {
#pragma unroll
  for (int i = 0; i < 8; ++i) v[i] = swapsum32(v[i], v[i + 8]);
#pragma unroll
  for (int i = 0; i < 4; ++i) v[i] = swapsum16(v[i], v[i + 4]);
#pragma unroll
  for (int i = 0; i < 4; ++i) v[i] += dpp<D_ROR8>(v[i]);
  return f32x4{v[0], v[1], v[2], v[3]};
}

#define STAMP_KEEP
#define STAMP(i)
#define STAMP_FLUSH(n)

#define GM_PS0 36
#define GM_PS1 16
#define GM_SL_HI 6
#define GM_SL_MID 2
#define GM_S2_DELAY 6
#define GM_TGATE 700
#define GM_CODE_LINES 119
#define BAD4(a) ((((a)[0][1] ^ (a)[0][3]) ^ tgx) | (((a)[1][1] ^ (a)[1][3]) ^ tgx) | (((a)[2][1] ^ (a)[2][3]) ^ tgx) | (((a)[3][1] ^ (a)[3][3]) ^ tgx))
#define USUM4(a) ((unpack4((a)[0], tag) + unpack4((a)[1], tag)) + (unpack4((a)[2], tag) + unpack4((a)[3], tag)))

#define GM_K0 8
#define GM_ONEPOLL 0
#define GM_OUTAUX 0
#define GM_FTMAP 2
#define GM_ETOUCH 0
#define GM_LTOUCH 2
#define GM_F1AT 4
#define GM_HGATE 740
#define GM_V0 0
#define GM_WGATE 520
#define GM_WLATE 1
#define GM_W2GATE 0
#define GM_TLB 1
#define GM_HSL 2
#define GM_SL_LO 0
#define GM_GC (grp ^ 1)
#define GM_GQ grp
#define GM_WSIG 1

__global__ void __launch_bounds__(NTHR)
fused_kernel(const u16* x, const u16* wq_, unsigned char* ws, const u16* gw, const u16* Kc, const u16* wg, const u16* wu,
             const u16* Vc, const u16* wo, const u16* wd, u16* out) {
  __shared__ __attribute__((aligned(16))) unsigned char lds_raw[LDS_BYTES];
  unsigned long long p_x, p_wq, p_ws, pc0;
  asm volatile("s_load_dwordx2 %0, %4, 0x0\n\ts_load_dwordx2 %1, %4, 0x8\n\ts_load_dwordx2 %2, %4, 0x10\n\ts_getpc_b64 %3"
               : "=&s"(p_x), "=&s"(p_wq), "=&s"(p_ws), "=&s"(pc0)
               : "s"(__builtin_amdgcn_kernarg_segment_ptr()));
  SB();
  LAS unsigned char* L = (LAS unsigned char*)lds_raw;
  LAS unsigned* cnt = (LAS unsigned*)(L + L_CNT);
  const int tid = threadIdx.x, lane = tid & 63;
  const int w = __builtin_amdgcn_readfirstlane(tid >> 6);
  const int bid = blockIdx.x, grp = bid & 7, r = bid >> 3;
  unsigned codew, epoch;
  v2u xq0, xq1;
  v4u big[4];
  {
    const int qrow = 128 * (r >> 3) + 32 * w + 4 * (lane >> 3);
    const unsigned qo = (qrow * HD + 512 * ((GM_GQ) & 7) + 64 * (r & 7) + 8 * (lane & 7)) * 2;
    const int cl = 4 * r + (lane & 3);
    const unsigned coff = (cl < GM_CODE_LINES ? cl : 0) * 128;
    asm volatile(
        "s_cmp_lg_u32 %[w], 9\n\ts_cbranch_scc1 1f\n\t"
        "global_load_dword %[cw], %[coff], %[pc]\n\t"
        "s_waitcnt lgkmcnt(0)\n\t"
        "global_load_dword %[ep], %[vz], %[pws] sc1\n"
        "1:\n\ts_cmp_gt_u32 %[w], 3\n\ts_cbranch_scc1 2f\n\t"
        "s_waitcnt lgkmcnt(0)\n\t"
        "global_load_dwordx4 %[b0], %[o0], %[pb] nt\n\t"
        "global_load_dwordx4 %[b1], %[o1], %[pb] nt\n\t"
        "global_load_dwordx4 %[b2], %[o2], %[pb] nt\n\t"
        "global_load_dwordx4 %[b3], %[o3], %[pb] nt\n\t"
        "global_load_dwordx2 %[xa], %[xoff], %[px]\n\t"
        "global_load_dwordx2 %[xb], %[xoff], %[px] offset:1024\n"
        "2:"
        : [cw] "=&v"(codew), [ep] "=&v"(epoch), [xa] "=&v"(xq0), [xb] "=&v"(xq1), [b0] "=&v"(big[0]), [b1] "=&v"(big[1]),
          [b2] "=&v"(big[2]), [b3] "=&v"(big[3])
        : [w] "s"(w), [coff] "v"(coff), [pc] "s"(pc0 & ~127ull), [xoff] "v"(qrow * 2), [px] "s"(p_x), [vz] "v"(0), [pws] "s"(p_ws),
          [pb] "s"(p_wq), [o0] "v"(qo), [o1] "v"(qo + HD * 2), [o2] "v"(qo + HD * 4), [o3] "v"(qo + HD * 6)
        : "memory", "scc");
  }
#define WGBAR_A()                                                   \
  do {                                                              \
    WGBAR();                                                        \
    asm volatile("" ::"s"(p_x), "s"(p_wq), "s"(p_ws) STAMP_KEEP);   \
  } while (0)
#define PRO_WAIT(N)                                             \
  do {                                                          \
    SB();                                                       \
    asm volatile("s_waitcnt vmcnt(" #N ")" ::: "memory");       \
    SB();                                                       \
  } while (0)
  const int sw = grp * 2 + (r >> 4);
  const size_t kvbase = ((size_t)((r >> 4) * 8 + grp) * SK + (SK - WINDOW)) * DH + (4 * (r & 15) + (w & 3)) * 512;
  const int gc = (GM_GC) & 7;
  const int j = lane >> 4, n = lane & 15;
#define MAKE_RS() asm volatile("" : "+s"(ws)); const __amdgpu_buffer_rsrc_t rs = __builtin_amdgcn_make_buffer_rsrc(ws, 0, WS_END, 0x00020000)
  const f32x4 zero4 = {0.f, 0.f, 0.f, 0.f};
  constexpr float LOG2E = 1.4426950408889634f;

  if (w < 4) {
    WGBAR_A();
    PRO_WAIT(0);
    asm volatile("" : "+v"(xq0), "+v"(xq1), "+v"(big[0]), "+v"(big[1]), "+v"(big[2]), "+v"(big[3]));
    {
      float v[16];
#pragma unroll
      for (int e = 0; e < 8; ++e) {
        const unsigned p01 = permsel(big[1][e >> 1], big[0][e >> 1], e), p23 = permsel(big[3][e >> 1], big[2][e >> 1], e);
        v[e] = dot2(p23, xq0[1], dot2(p01, xq0[0], 0.f));
        v[8 + e] = dot2(p23, xq1[1], dot2(p01, xq1[0], 0.f));
      }
      const f32x4 a = reduce16(v);
      if ((lane & 8) == 0) *(LAS f32x4*)(L + L_QRED + ((w * 2 + (lane >> 5)) * 64 + 8 * (lane & 7) + 4 * ((lane >> 4) & 1)) * 4) = a;
    }
    lds_signal(cnt + 6);
    STAMP(1);
    MAKE_RS();
    v4u vf[16];
    if (GM_V0 > 0) lds_wait(cnt + 8, 1);
#pragma unroll
    for (int i = 0; i < GM_V0; ++i) vf[i] = ldnt(Vc + kvbase + (i ^ sw) * 32768 + lane * 8);
    SB();
    lds_wait(cnt + 7, 2);
#pragma unroll
    for (int i = GM_V0; i < 16; ++i) vf[i] = ldnt(Vc + kvbase + (i ^ sw) * 32768 + lane * 8);
    SB();
    lds_signal(cnt + 9);
    STAMP(2);
    v4u be[8];
#pragma unroll
    for (int q = 0; q < 4; ++q) {
#pragma unroll
      for (int e = 0; e < 8; ++e) be[e][q] = permsel(vf[2 * q + 1][e >> 1], vf[2 * q][e >> 1], e);
      SB();
    }
#pragma unroll
    for (int e = 0; e < 8; ++e) asm volatile("" : "+v"(be[e]));
    STAMP(3);
    lds_wait(cnt + 5, 4);
    STAMP(4);
    __builtin_amdgcn_s_setprio(3);
    const v4u pa0 = *(LAS v4u*)(L + L_P + (w * 2) * 1024 + lane * 16), pa1 = *(LAS v4u*)(L + L_P + (w * 2 + 1) * 1024 + lane * 16);
    const float Ls = sum4(*(LAS f32x4*)(L + L_LSUM + w * 16));
    const float Mh = max4(*(LAS f32x4*)(L + L_WMAX + w * 16));
    const unsigned tag = cnt[12];
    f32x4 oacc[8];
#pragma unroll
    for (int e = 0; e < 8; ++e) oacc[e] = mfma32(pa0, be[e], zero4);
    float inv = __builtin_amdgcn_rcpf(Ls);
    const unsigned lse = __float_as_uint(fmaf(Mh, LOG2E, __builtin_amdgcn_logf(Ls)));
    unsigned lA = lse ^ tag, lB = lse ^ tag_b(tag);
    int s2dst = lane < 32 ? S2_OFF + (grp * 32 + r) * 2048 + w * 512 + lane * 16 : OOB;
    int src = L_OP + (lane >> 5) * 2 * OP_W + (lane & 1) * 1152 + (w * 16 + ((lane & 31) >> 1)) * 16;
    asm volatile("" : "+v"(inv), "+v"(lA), "+v"(lB), "+v"(s2dst), "+v"(src));
    SB();
    STAMP(5);
#pragma unroll
    for (int q = 0; q < 4; ++q) {
#pragma unroll
      for (int e = 0; e < 8; ++e) be[e][q] = permsel(vf[8 + 2 * q + 1][e >> 1], vf[8 + 2 * q][e >> 1], e);
      if (q < 3) SB();
    }
#pragma unroll
    for (int e = 0; e < 8; ++e) oacc[e] = mfma32(pa1, be[e], oacc[e]);
    *(LAS f32x4*)(L + L_OP + w * OP_W + lane * 16) = f32x4{oacc[0][0], oacc[1][0], oacc[2][0], oacc[3][0]};
    *(LAS f32x4*)(L + L_OP + w * OP_W + 1152 + lane * 16) = f32x4{oacc[4][0], oacc[5][0], oacc[6][0], oacc[7][0]};
    STAMP(7);
    lds_signal(cnt + 0);
    lds_wait_fast(cnt + 0, 4);
    {
      const f32x4 t = *(LAS f32x4*)(L + src) + *(LAS f32x4*)(L + src + OP_W);
      const float a0 = swapsum32(t[0], t[0]), a1 = swapsum32(t[1], t[1]), a2 = swapsum32(t[2], t[2]), a3 = swapsum32(t[3], t[3]);
      st_sc1(rs, s2dst, v4u{cvtpk(a0 * inv, a1 * inv), lA, cvtpk(a2 * inv, a3 * inv), lB});
    }
    lds_signal(cnt + 10);
    STAMP(8);
    STAMP_FLUSH(9);
    return;
  }

  if (w < 8) {
    __builtin_amdgcn_s_setprio(2);
    const int k = w - 4;
    const unsigned long long t_gate = __builtin_amdgcn_s_memrealtime() + GM_HGATE;
    const u16* kp = Kc + kvbase + lane * 8;
    WGBAR_A();
    v4u kf[16];
#pragma unroll
    for (int i = 0; i < GM_K0; ++i) kf[i] = ldnt(kp + (i ^ sw) * 32768);
    SB();
    STAMP(1);
    lds_wait(cnt + 8, 1);
#pragma unroll
    for (int i = GM_K0; i < 16; ++i) kf[i] = ldnt(kp + (i ^ sw) * 32768);
    SB();
    STAMP(2);
    LAS unsigned char* Kl = L + L_K + k * 16384;
#pragma unroll
    for (int i = 0; i < 16; ++i) {
      const int key = 4 * i + j;
      *(LAS v4u*)(Kl + key * 256 + ((n ^ (key & 15)) << 4)) = kf[i];
    }
    v4u ka[4][4];
    {
      const int kin = 4 * (n & 3) + (n >> 2);
#pragma unroll
      for (int t = 0; t < 4; ++t)
#pragma unroll
        for (int st = 0; st < 4; ++st) ka[t][st] = *(LAS v4u*)(Kl + (16 * t + kin) * 256 + (((4 * st + j) ^ kin) << 4));
    }
    STAMP(3);
    lds_wait(cnt + 2, 2);
    STAMP(4);
    v4u qf[4];
#pragma unroll
    for (int st = 0; st < 4; ++st) qf[st] = *(LAS v4u*)(L + L_Q + (n >> 2) * 256 + (4 * st + j) * 16);
    f32x4 sc[4];
#pragma unroll
    for (int t = 0; t < 4; ++t) sc[t] = zero4;
#pragma unroll
    for (int st = 0; st < 4; ++st)
#pragma unroll
      for (int t = 0; t < 4; ++t) sc[t] = mfma32(ka[t][st], qf[st], sc[t]);
#pragma unroll
    for (int t = 0; t < 4; ++t) {
      const unsigned a = cvtpk(sc[t][0], sc[t][1]), b = cvtpk(sc[t][2], sc[t][3]);
      const unsigned c = cvtpk(bflo(a) * 0.08838834764831845f, bfhi(a) * 0.08838834764831845f);
      const unsigned d = cvtpk(bflo(b) * 0.08838834764831845f, bfhi(b) * 0.08838834764831845f);
      sc[t] = f32x4{bflo(c), bfhi(c), bflo(d), bfhi(d)};
    }
    const float mx = xrow_max(fmaxf(fmaxf(max4(sc[0]), max4(sc[1])), fmaxf(max4(sc[2]), max4(sc[3]))));
    if (lane < 16 && (lane & 3) == 0) *(LAS float*)(L + L_WMAX + (lane + k) * 4) = mx;
    lds_signal(cnt + 1);
    lds_wait(cnt + 1, 4);
    STAMP(5);
    const float Mc = LOG2E * max4(*(LAS f32x4*)(L + L_WMAX + (n >> 2) * 16));
    v4u pa[2];
#pragma unroll
    for (int t = 0; t < 4; ++t) {
      float p[4];
#pragma unroll
      for (int i = 0; i < 4; ++i) p[i] = __builtin_amdgcn_exp2f(fmaf(sc[t][i], LOG2E, -Mc));
      pa[t >> 1][2 * (t & 1)] = cvtpk(p[0], p[1]);
      pa[t >> 1][2 * (t & 1) + 1] = cvtpk(p[2], p[3]);
    }
    *(LAS v4u*)(L + L_P + (k * 2) * 1024 + lane * 16) = pa[0];
    *(LAS v4u*)(L + L_P + (k * 2 + 1) * 1024 + lane * 16) = pa[1];
    const v4u ones = {0x3F803F80u, 0x3F803F80u, 0x3F803F80u, 0x3F803F80u};
    const f32x4 ls = mfma32(pa[1], ones, mfma32(pa[0], ones, zero4));
    if (n == 0) *(LAS float*)(L + L_LSUM + (j * 4 + k) * 4) = ls[0];
    lds_signal(cnt + 5);
    STAMP(6);
    if (k == 0) {
      MAKE_RS();
      const unsigned tag = cnt[12], tgx = tag ^ tag_b(tag);
      const int cc = 224 * gc + 56 * (r >> 3) + 2 * ((lane & 31) < 28 ? (lane & 31) : 27);
      const int hoff = F1_OFF + ((lane >> 5) * 28 + (cc >> 6)) * 2048 + (cc & 63) * 8;
      const float omg = bfround(1.0f - __uint_as_float(cnt[13 + (lane >> 5)]));
      lds_wait(cnt + 10, 4);
      wait_until(t_gate);
      v4u hg[4];
      bool hh[4];
#pragma unroll
      for (int p = 0; p < 4; ++p) hg[p] = ld_sc1(rs, hoff + p * 512);
#pragma unroll
      for (int p = 0; p < 4; ++p) hh[p] = (hg[p][1] ^ hg[p][3]) == tgx;
      bool done = __all(hh[0] & hh[1] & hh[2] & hh[3]);
#pragma nounroll
      for (unsigned spins = 0; !done && spins < SPIN_CAP; ++spins) {
        __builtin_amdgcn_s_sleep(GM_HSL);
        CBAR();
        v4u t[4];
#pragma unroll
        for (int p = 0; p < 4; ++p) t[p] = ld_sc1(rs, hh[p] ? OOB : hoff + p * 512);
        SB();
#pragma unroll
        for (int p = 0; p < 4; ++p) {
          hg[p][0] = hh[p] ? hg[p][0] : t[p][0];
          hg[p][1] = hh[p] ? hg[p][1] : t[p][1];
          hg[p][2] = hh[p] ? hg[p][2] : t[p][2];
          hh[p] = hh[p] | ((t[p][1] ^ t[p][3]) == tgx);
        }
        done = __all(hh[0] & hh[1] & hh[2] & hh[3]);
      }
      STAMP(7);
      const f32x4 hsum = USUM4(hg);
      float hv2[2];
#pragma unroll
      for (int c = 0; c < 2; ++c) {
        auto sres = __builtin_amdgcn_permlane32_swap(__float_as_uint(hsum[c]), __float_as_uint(hsum[2 + c]), false, false);
        const float gb = bfround(__uint_as_float(sres[0]));
        const float si = bfround(gb * __builtin_amdgcn_rcpf(1.0f + __expf(-gb)));
        hv2[c] = bfround(si * bfround(__uint_as_float(sres[1]))) * omg;
      }
      unsigned hp = cvtpk(hv2[0], hv2[1]);
      if (!done) hp = 0x7FC07FC0u;
      *(LAS unsigned*)(L + L_H + lane * 4) = hp;
      lds_signal(cnt + 3);
      STAMP(8);
    }
    STAMP_FLUSH(9);
    return;
  }

  {
    __builtin_amdgcn_s_setprio(3);
    const int tw = w - 8;
    MAKE_RS();
    const unsigned long long t_0 = __builtin_amdgcn_s_memrealtime();
    const int cblk = r & 7, hrel = r >> 3;
    const u16* wop = wo + (size_t)(512 * gc + 128 * hrel + 32 * tw + 4 * (lane >> 3)) * DM + 64 * cblk + 8 * (lane & 7);
    const u16* wdp = wd + (size_t)(224 * gc + 56 * hrel + 14 * tw + (lane < 56 ? 2 * (lane >> 3) : 12)) * DM + 64 * cblk + 8 * (lane & 7);
    int s2off = S2_OFF + (gc * 32 + (lane >> 3)) * 2048 + hrel * 512 + tw * 128 + (lane & 7) * 16;
    int s3off = S3_OFF + ((bid >> 2) * 32 + (lane >> 3)) * 512 + (bid & 3) * 128 + (lane & 7) * 16;
    asm volatile("" : "+v"(s2off), "+v"(s3off));
    v4u wfr[4], dfr[2];
#define ISSUE_WO_WD()                                                             \
  do {                                                                            \
    STAMP(5);                                                                     \
    _Pragma("unroll") for (int i = 0; i < 4; ++i) wfr[i] = ldnt(wop + i * DM);    \
    dfr[0] = ldnt(wdp);                                                           \
    dfr[1] = ldnt(wdp + DM);                                                      \
    SB();                                                                         \
  } while (0)
#define TAGS_LDS()                                                                \
  do {                                                                            \
    tag = cnt[12];                                                                \
    tagB = tag_b(tag);                                                            \
    tgx = tag ^ tagB;                                                             \
  } while (0)
#define TAGS()                                                                    \
  do {                                                                            \
    asm volatile("" : "+v"(epoch));                                               \
    unsigned tg = epoch * 1664525u + 1013904223u;   \
    if (tg == 0u) tg = 1013904223u;                 \
    tag = tg;                                                                     \
    tagB = tag_b(tg);                                                             \
    tgx = tag ^ tagB;                                                             \
    asm volatile("" : "+v"(tag), "+v"(tagB), "+v"(tgx));                          \
  } while (0)
    unsigned tag, tagB, tgx;
    bool alive = true;
    const bool has_ft = r < 28;
    const int ft = grp * 28 + (r < 28 ? r : 0);
    const int fmat = ft >= 112, fcb = (ft - 112 * fmat) >> 2, frb = ft & 3;
    if (tw < 2) {
      if (tw == 1) {
        if (lane < 16) cnt[lane] = 0u;
        WGBAR_A();
        PRO_WAIT(0);
        asm volatile("" ::"v"(codew));
        TAGS();
        if (lane == 0) cnt[12] = tag;
        STAMP(1);
        lds_wait(cnt + 6, 4);
        f32x4 a = *(LAS f32x4*)(L + L_QRED + (lane >> 5) * 1024 + (lane & 31) * 16) + *(LAS f32x4*)(L + L_QRED + (lane >> 5) * 1024 + 512 + (lane & 31) * 16);
        a = f32x4{swapsum32(a[0], a[0]), swapsum32(a[1], a[1]), swapsum32(a[2], a[2]), swapsum32(a[3], a[3])};
        st_sc1(rs, lane < 32 ? QG_OFF + ((((GM_GQ) & 7) * 8 + (r & 7)) * 4 + (r >> 3)) * 512 + lane * 16 : OOB, pack4(a, tag, tagB));
        lds_signal(cnt + 8);
        STAMP(2);
      } else {
        const v4u gv = *(const GAS v4u*)(gw + 8 * lane);
        const v4u xa = *(const GAS v4u*)(x + 8 * lane), xb = *(const GAS v4u*)(x + DM + 8 * lane);
        WGBAR_A();
        float a0 = 0.f, a1 = 0.f;
#pragma unroll
        for (int q = 0; q < 4; ++q) {
          a0 += bfround(bflo(gv[q]) * bflo(xa[q])) + bfround(bfhi(gv[q]) * bfhi(xa[q]));
          a1 += bfround(bflo(gv[q]) * bflo(xb[q])) + bfround(bfhi(gv[q]) * bfhi(xb[q]));
        }
        float a = swapsum32(a0, a1);
        a = swapsum16(a, a);
        a += dpp<D_ROR8>(a);
        a += dpp<D_ROR4>(a);
        a += dpp<D_XOR2>(a);
        a += dpp<D_XOR1>(a);
        const float gt = bfround(__builtin_amdgcn_rcpf(1.0f + __expf(-bfround(a))));
        if ((lane & 31) == 0) cnt[13 + (lane >> 5)] = __float_as_uint(gt);
        STAMP(1);
        lds_wait(cnt + 8, 1);
        TAGS_LDS();
      }
      const int qoff = QG_OFF + (grp * 8 + 4 * tw + (lane >> 4)) * 2048 + (r >> 4) * 256 + (lane & 15) * 16;
      v4u pq[2][4];
#pragma unroll
      for (int kk = 0; kk < 2; ++kk) {
        if (kk == 0) __builtin_amdgcn_s_sleep(GM_PS0);
        else __builtin_amdgcn_s_sleep(GM_PS1);
        CBAR();
#pragma unroll
        for (int p = 0; p < 4; ++p) pq[kk][p] = ld_sc1(rs, (GM_ONEPOLL && kk == 1 ? OOB - 4096 : qoff) + p * 512);
        SB();
        if (kk == 0) lds_signal(cnt + 7);
      }
      STAMP(3);
      f32x4 qv;
      if (__all(BAD4(pq[0]) == 0u)) {
        qv = USUM4(pq[0]);
      } else {
        bool done = __all(BAD4(pq[1]) == 0u);
        unsigned spins = 0;
#pragma nounroll
        while (!done && ++spins < SPIN_CAP) {
          CBAR();
#pragma unroll
          for (int p = 0; p < 4; ++p) pq[1][p] = ld_sc1(rs, qoff + p * 512);
          done = __all(BAD4(pq[1]) == 0u);
        }
        alive = done;
        qv = USUM4(pq[1]);
      }
      *(LAS v2u*)(L + L_Q + tw * 512 + lane * 8) = v2u{cvtpk(qv[0], qv[1]), cvtpk(qv[2], qv[3])};
      lds_signal(cnt + 2);
      STAMP(4);
      asm volatile("" ::"v"(pq[1][0]), "v"(pq[1][1]), "v"(pq[1][2]), "v"(pq[1][3]));
      STAMP(6);
    } else {
      const int frow = 128 * frb + 64 * (tw & 1) + 8 * (lane >> 3);
      const v4u xa = *(const GAS v4u*)(x + frow), xb = *(const GAS v4u*)(x + DM + frow);
      const u16* wfp = (fmat ? wu : wg) + (size_t)frow * FF + 64 * fcb + 8 * (lane & 7);
      unsigned tl;
      {
        const u16* tp = tw == 2 ? (lane < 16 ? Vc : Kc) + kvbase + (lane & 15) * 32768
                                : (lane < 8 ? wfp + (size_t)0 : lane < 16 ? wop : wdp);
        const bool act = tw == 2 ? lane < GM_TLB : (lane & 7) == 0 && lane < 24;
        tl = 0;
        if (act) tl = __builtin_nontemporal_load((const GAS unsigned*)tp);
      }
      WGBAR_A();
      asm volatile("" ::"v"(xa), "v"(xb));
      asm volatile("" ::"v"(tl));
      lds_wait(cnt + 9, 4);
      TAGS_LDS();
      v4u tt[3];
      tt[0] = ld_sc1(rs, GM_LTOUCH ? S2_OFF + ((GM_LTOUCH == 2 ? gc : grp) * 32 + r) * 2048 + (tw & 1) * 1024 + lane * 16 : OOB);
      tt[1] = ld_sc1(rs, bid < 32 ? s3off + (tw & 1) * 8192 : OOB);
      tt[2] = ld_sc1(rs, bid < 32 ? s3off + (tw & 1) * 8192 + 4096 : OOB);
      SB();
      wait_until(t_0 + GM_WGATE);
      STAMP(4);
      v4u wf[8];
#pragma unroll
      for (int i = 0; i < 8; ++i) wf[i] = ldnt(wfp + (has_ft ? i * FF : 0));
      SB();
      STAMP(6);
      float v[16];
#pragma unroll
      for (int e = 0; e < 8; ++e) {
        v[e] = 0.f; v[8 + e] = 0.f;
#pragma unroll
        for (int q = 0; q < 4; ++q) {
          const unsigned pk = permsel(wf[2 * q + 1][e >> 1], wf[2 * q][e >> 1], e);
          v[e] = dot2(pk, xa[q], v[e]);
          v[8 + e] = dot2(pk, xb[q], v[8 + e]);
        }
      }
      asm volatile("" ::"v"(tt[0]), "v"(tt[1]), "v"(tt[2]));
      const f32x4 fa = reduce16(v);
      if ((lane & 8) == 0) *(LAS f32x4*)(L + L_FRED + (((tw & 1) * 2 + (lane >> 5)) * 64 + 8 * (lane & 7) + 4 * ((lane >> 4) & 1)) * 4) = fa;
      const bool last = lds_signal_ret(cnt + 4) == 1u;
      const f32x2 a0 = *(LAS f32x2*)(L + L_FRED + (lane & 31) * 8) + *(LAS f32x2*)(L + L_FRED + 512 + (lane & 31) * 8);
      const f32x2 a1 = *(LAS f32x2*)(L + L_FRED + 256 + (lane & 31) * 8) + *(LAS f32x2*)(L + L_FRED + 768 + (lane & 31) * 8);
      asm volatile("buffer_store_dwordx4 %0, %1, %2, 0 offen sc1\n\ts_nop 1" ::"v"(pack4(f32x4{a0[0], a0[1], a1[0], a1[1]}, tag, tagB)),
                   "v"(last && has_ft && lane < 32 ? F1_OFF + ft * 512 + lane * 16 : OOB), "s"(rs) : "memory");
      STAMP(3);
    }
    lds_wait(cnt + 10, 4);
    STAMP(7);
    wait_until(t_0 + GM_W2GATE);
    ISSUE_WO_WD();
    const float gate = __uint_as_float(cnt[13 + (lane >> 5)]);
    unsigned o0[4], o1[4], o2[4];
    const int rho = 2 * j + ((n >> 2) & 1);
    int ahr = rho < 7 ? L_H + (n >> 3) * 128 + tw * 28 + rho * 4 : L_CNT + 60;
    int oaw = L_OA + tw * 128 + (lane >> 5) * 64 + (lane & 7) * 8 + ((lane >> 4) & 1) * 4;
    int oar = L_OA + tw * 128 + (n >> 3) * 64 + rho * 8;
    int redw = (j & 1) == (n >> 3) ? L_RED + ((tw * 2 + (j & 1)) * 2 + (j >> 1)) * 256 + (n & 7) * 32 : L_DUMP + lane * 32;
    v4u wb[8];
    {
      bool ho[4];
      v4u od[4];
      wait_until(t_0 + GM_TGATE);
#pragma unroll
      for (int i = 0; i < 4; ++i) od[i] = ld_sc1(rs, s2off + i * 8 * 2048);
      SB();
#pragma unroll
      for (int e = 0; e < 8; ++e) {
        wb[e][0] = permsel(wfr[1][e >> 1], wfr[0][e >> 1], e);
        wb[e][1] = permsel(wfr[3][e >> 1], wfr[2][e >> 1], e);
        wb[e][2] = lane < 56 ? permsel(dfr[1][e >> 1], dfr[0][e >> 1], e) : 0u;
        wb[e][3] = 0u;
        asm volatile("" : "+v"(wb[e]));
      }
      asm volatile("" : "+v"(oaw), "+v"(oar), "+v"(redw), "+v"(ahr));
      SB();
      STAMP(15);
#pragma unroll
      for (int i = 0; i < 4; ++i) {
        ho[i] = (od[i][1] ^ od[i][3]) == tgx;
        o0[i] = od[i][0]; o1[i] = od[i][1]; o2[i] = od[i][2];
      }
      unsigned spins = 0;
#pragma nounroll
      while (true) {
        const int miss = __builtin_popcountll(__ballot(!ho[0])) + __builtin_popcountll(__ballot(!ho[1])) +
                         __builtin_popcountll(__ballot(!ho[2])) + __builtin_popcountll(__ballot(!ho[3]));
        if (miss == 0) break;
        if (++spins >= SPIN_CAP) { alive = false; break; }
        if (miss > 128) __builtin_amdgcn_s_sleep(GM_SL_HI);
        else if (miss > 40) __builtin_amdgcn_s_sleep(GM_SL_MID);
        else if (GM_SL_LO) __builtin_amdgcn_s_sleep(GM_SL_LO);
        CBAR();
#pragma unroll
        for (int i = 0; i < 4; ++i) od[i] = ld_sc1(rs, ho[i] ? OOB : s2off + i * 8 * 2048);
        SB();
#pragma unroll
        for (int i = 0; i < 4; ++i) {
          o0[i] = ho[i] ? o0[i] : od[i][0];
          o1[i] = ho[i] ? o1[i] : od[i][1];
          o2[i] = ho[i] ? o2[i] : od[i][2];
          ho[i] = ho[i] | ((od[i][1] ^ od[i][3]) == tgx);
        }
      }
    }
    STAMP(9);
    {
      float ls[4], ov[10];
#pragma unroll
      for (int i = 0; i < 4; ++i) ls[i] = __uint_as_float(o1[i] ^ tag);
      float mxb[2];
      {
        auto s0 = __builtin_amdgcn_permlane32_swap(__float_as_uint(fmaxf(ls[0], ls[1])), __float_as_uint(fmaxf(ls[2], ls[3])), false, false);
        float m = fmaxf(__uint_as_float(s0[0]), __uint_as_float(s0[1]));
        auto s1 = __builtin_amdgcn_permlane16_swap(__float_as_uint(m), __float_as_uint(m), false, false);
        m = fmaxf(__uint_as_float(s1[0]), __uint_as_float(s1[1]));
        m = fmaxf(m, dpp<D_ROR8>(m));
        auto s2 = __builtin_amdgcn_permlane32_swap(__float_as_uint(m), __float_as_uint(m), false, false);
        mxb[0] = __uint_as_float(s2[0]);
        mxb[1] = __uint_as_float(s2[1]);
      }
#pragma unroll
      for (int bb = 0; bb < 2; ++bb) {
        const float w0 = __builtin_amdgcn_exp2f(ls[2 * bb] - mxb[bb]), w1 = __builtin_amdgcn_exp2f(ls[2 * bb + 1] - mxb[bb]);
        ov[4 * bb + 0] = w0 * bflo(o0[2 * bb]) + w1 * bflo(o0[2 * bb + 1]);
        ov[4 * bb + 1] = w0 * bfhi(o0[2 * bb]) + w1 * bfhi(o0[2 * bb + 1]);
        ov[4 * bb + 2] = w0 * bflo(o2[2 * bb]) + w1 * bflo(o2[2 * bb + 1]);
        ov[4 * bb + 3] = w0 * bfhi(o2[2 * bb]) + w1 * bfhi(o2[2 * bb + 1]);
        ov[8 + bb] = w0 + w1;
      }
#pragma unroll
      for (int i = 0; i < 4; ++i) ov[i] = swapsum32(ov[i], ov[i + 4]);
      float W = swapsum32(ov[8], ov[9]);
      ov[0] = swapsum16(ov[0], ov[2]);
      ov[1] = swapsum16(ov[1], ov[3]);
      W = swapsum16(W, W);
      ov[0] += dpp<D_ROR8>(ov[0]);
      ov[1] += dpp<D_ROR8>(ov[1]);
      W += dpp<D_ROR8>(W);
      float sc = gate * __builtin_amdgcn_rcpf(W);
      if (!alive) sc = __uint_as_float(0x7FC00000u);
      *(LAS unsigned*)(L + oaw) = cvtpk(sc * ov[0], sc * ov[1]);
      lds_wait(cnt + 3, 1);
      const unsigned ah = *(LAS unsigned*)(L + ahr);
      const v2u ao = *(LAS v2u*)(L + oar);
      const v4u am = {ao[0], ao[1], ah, 0u};
      float res[8];
#pragma unroll
      for (int e = 0; e < 8; ++e) res[e] = mfma32(am, wb[e], zero4)[0];
      *(LAS f32x4*)(L + redw) = f32x4{res[0], res[1], res[2], res[3]};
      *(LAS f32x4*)(L + redw + 16) = f32x4{res[4], res[5], res[6], res[7]};
    }
    STAMP(10);
    WGBAR();
    if (tw == 0) {
      f32x4 a = zero4;
#pragma unroll
      for (int t = 0; t < 4; ++t) a += *(LAS f32x4*)(L + L_RED + ((lane >> 5) * 4 + t) * 512 + (lane & 31) * 16);
      const f32x4 s = {swapsum32(a[0], a[0]), swapsum32(a[1], a[1]), swapsum32(a[2], a[2]), swapsum32(a[3], a[3])};
      if (lane < 32) st_sc1(rs, S3_OFF + (cblk * 32 + 4 * gc + hrel) * 512 + lane * 16, pack4(s, tag, tagB));
      STAMP(11);
    } else if (tw == 1 && bid < 32) {
      v4u pd[4];
      unsigned spins = 0;
      bool done;
#pragma nounroll
      do {
        CBAR();
#pragma unroll
        for (int i = 0; i < 4; ++i) pd[i] = ld_sc1(rs, s3off + i * 4096);
        SB();
        done = __all(BAD4(pd) == 0u);
      } while (!done && ++spins < SPIN_CAP);
      alive = alive && done;
      STAMP(12);
      const f32x4 a = USUM4(pd);
      float b0 = swapsum32(a[0], a[2]), b1 = swapsum32(a[1], a[3]);
      b0 = swapsum16(b0, b0);
      b1 = swapsum16(b1, b1);
      b0 += dpp<D_ROR8>(b0);
      b1 += dpp<D_ROR8>(b1);
      unsigned pk = cvtpk(b0, b1);
      if (!alive) pk = 0x7FC07FC0u;
      if ((lane & 24) == 0)
        __builtin_amdgcn_raw_buffer_store_b32(pk, __builtin_amdgcn_make_buffer_rsrc(out, 0, 2048, 0x00020000),
                                              (((bid >> 1) & 1) * DM + 64 * (bid >> 2) + 32 * (bid & 1) + 4 * (lane & 7) + 2 * (lane >> 5)) * 2, 0, GM_OUTAUX);
      if (bid == 0 && lane == 0) __hip_atomic_store((GAS unsigned*)(ws + EPOCH_OFF), tag, RLX_AGENT);
      STAMP(13);
    }
    STAMP_FLUSH(14);
  }
}

}

extern "C" void kernel_launch(void* const* d_in, const int* in_sizes, int n_in, void* d_out, int out_size, void* d_ws,
                              size_t ws_size, hipStream_t stream) {
  using gm::u16;
  (void)in_sizes; (void)n_in; (void)out_size; (void)ws_size;
  gm::fused_kernel<<<gm::NWG, gm::NTHR, 0, stream>>>(
      (const u16*)d_in[0], (const u16*)d_in[3], (unsigned char*)d_ws, (const u16*)d_in[9], (const u16*)d_in[1],
      (const u16*)d_in[6], (const u16*)d_in[7], (const u16*)d_in[2], (const u16*)d_in[4], (const u16*)d_in[8], (u16*)d_out);
}
```

```cpp
#include <hip/hip_runtime.h>
#include <hip/hip_bf16.h>
#include <stdint.h>

namespace gm {

typedef unsigned v4u __attribute__((ext_vector_type(4)));
typedef unsigned v2u __attribute__((ext_vector_type(2)));
typedef float f32x4 __attribute__((ext_vector_type(4)));
typedef float f32x2 __attribute__((ext_vector_type(2)));
typedef __bf16 bf16x8_t __attribute__((ext_vector_type(8)));
typedef __bf16 bf16x2_t __attribute__((ext_vector_type(2)));
typedef unsigned short u16;

#define GAS __attribute__((address_space(1)))
#define LAS __attribute__((address_space(3)))
#define RLX_AGENT __ATOMIC_RELAXED, __HIP_MEMORY_SCOPE_AGENT

constexpr int SK = 32768, WINDOW = 4096, DH = 128, DM = 512, FF = 1792, HD = 4096;
constexpr int NWG = 256, NTHR = 768, NWAVES = 12;

constexpr int EPOCH_OFF = 0;
constexpr int QG_OFF = 4096;
constexpr int F1_OFF = QG_OFF + 256 * 512;
constexpr int S2_OFF = F1_OFF + 224 * 512;
constexpr int S3_OFF = S2_OFF + 8 * 32 * 2048;
constexpr int WS_END = S3_OFF + 8 * 32 * 512;

constexpr int L_CNT = 0;
constexpr int L_QRED = 64;
constexpr int L_FRED = L_QRED + 2048;
constexpr int L_Q = L_FRED + 1024;
constexpr int L_H = L_Q + 1024;
constexpr int L_OA = L_H + 256;
constexpr int L_WMAX = L_OA + 512;
constexpr int L_LSUM = L_WMAX + 64;
constexpr int L_RED = L_LSUM + 64;
constexpr int L_P = L_RED + 5120;
constexpr int L_OP = L_P + 8192;
constexpr int OP_W = 2304;
constexpr int L_K = L_OP + 4 * OP_W;
constexpr int L_DUMP = L_K;
constexpr int L_END = L_K + 4 * 16384;
constexpr int LDS_BYTES = 100 * 1024;
static_assert(L_END <= LDS_BYTES && (L_OP % 16) == 0 && (L_K % 16) == 0 && (L_RED % 16) == 0 && (L_P % 16) == 0, "lds map");

constexpr unsigned SPIN_CAP = 1u << 18;
constexpr int AUX_SC1 = 16;
constexpr int OOB = 0x7FFFFF00;

__device__ __forceinline__ v4u ldnt(const void* p) { return __builtin_nontemporal_load((const GAS v4u*)p); }
__device__ __forceinline__ float dot2(unsigned a, unsigned b, float c) {
  return __builtin_amdgcn_fdot2_f32_bf16(__builtin_bit_cast(bf16x2_t, a), __builtin_bit_cast(bf16x2_t, b), c, false);
}
__device__ __forceinline__ unsigned cvtpk(float lo, float hi) {
  bf16x2_t r = {(__bf16)lo, (__bf16)hi};
  return __builtin_bit_cast(unsigned, r);
}
__device__ __forceinline__ float bfround(float v) { return (float)(__bf16)v; }
__device__ __forceinline__ float bflo(unsigned u) { return __uint_as_float(u << 16); }
__device__ __forceinline__ float bfhi(unsigned u) { return __uint_as_float(u & 0xffff0000u); }
template <int CTRL> __device__ __forceinline__ float dpp(float x) {
  return __builtin_bit_cast(float, __builtin_amdgcn_mov_dpp(__builtin_bit_cast(int, x), CTRL, 0xf, 0xf, true));
}
constexpr int D_XOR1 = 0xB1, D_XOR2 = 0x4E, D_ROR8 = 0x128, D_ROR4 = 0x124;
__device__ __forceinline__ float swapsum32(float a, float b) {
  auto s = __builtin_amdgcn_permlane32_swap(__float_as_uint(a), __float_as_uint(b), false, false);
  return __uint_as_float(s[0]) + __uint_as_float(s[1]);
}
__device__ __forceinline__ float swapsum16(float a, float b) {
  auto s = __builtin_amdgcn_permlane16_swap(__float_as_uint(a), __float_as_uint(b), false, false);
  return __uint_as_float(s[0]) + __uint_as_float(s[1]);
}
__device__ __forceinline__ float xrow_max(float x) {
  auto s = __builtin_amdgcn_permlane16_swap(__float_as_uint(x), __float_as_uint(x), false, false);
  x = fmaxf(__uint_as_float(s[0]), __uint_as_float(s[1]));
  auto t = __builtin_amdgcn_permlane32_swap(__float_as_uint(x), __float_as_uint(x), false, false);
  return fmaxf(__uint_as_float(t[0]), __uint_as_float(t[1]));
}
__device__ __forceinline__ float max4(f32x4 a) { return fmaxf(fmaxf(a[0], a[1]), fmaxf(a[2], a[3])); }
__device__ __forceinline__ float sum4(f32x4 a) { return (a[0] + a[1]) + (a[2] + a[3]); }
__device__ __forceinline__ f32x4 mfma32(v4u a, v4u b, f32x4 c) {
  return __builtin_amdgcn_mfma_f32_16x16x32_bf16(__builtin_bit_cast(bf16x8_t, a), __builtin_bit_cast(bf16x8_t, b), c, 0, 0, 0);
}
typedef short s16x4 __attribute__((ext_vector_type(4)));
__device__ __forceinline__ f32x4 mfma16(v2u a, v2u b, f32x4 c) {
  return __builtin_amdgcn_mfma_f32_16x16x16bf16_1k(__builtin_bit_cast(s16x4, a), __builtin_bit_cast(s16x4, b), c, 0, 0, 0);
}
__device__ __forceinline__ unsigned permsel(unsigned hi, unsigned lo, int e) {
  return __builtin_amdgcn_perm(hi, lo, (e & 1) ? 0x07060302u : 0x05040100u);
}
__device__ __forceinline__ v4u ld_sc1(__amdgpu_buffer_rsrc_t rs, int off) {
  return __builtin_amdgcn_raw_buffer_load_b128(rs, off, 0, AUX_SC1);
}
__device__ __forceinline__ void st_sc1(__amdgpu_buffer_rsrc_t rs, int off, v4u v) {
  __builtin_amdgcn_raw_buffer_store_b128(v, rs, off, 0, AUX_SC1);
}
__device__ __forceinline__ unsigned tag_b(unsigned t) { return (t << 1) ^ ((t >> 31) ? 0x04C11DB7u : 0u); }
__device__ __forceinline__ v4u pack4(f32x4 v, unsigned tA, unsigned tB) {
  const unsigned u3 = __float_as_uint(v[3]);
  const unsigned a = __builtin_amdgcn_perm(__float_as_uint(v[0]), u3, 0x07060503u);
  const unsigned b = __builtin_amdgcn_perm(__float_as_uint(v[1]), u3, 0x07060502u);
  const unsigned c = __builtin_amdgcn_perm(__float_as_uint(v[2]), u3, 0x07060501u);
  return v4u{a, c ^ tA, b, c ^ tB};
}
__device__ __forceinline__ f32x4 unpack4(v4u p, unsigned tA) {
  const unsigned c = p[1] ^ tA;
  const unsigned t = __builtin_amdgcn_perm(p[0], p[2], 0x04000c0cu);
  const unsigned u3 = __builtin_amdgcn_perm(t, c, 0x0706000cu);
  return f32x4{__uint_as_float(p[0]), __uint_as_float(p[2]), __uint_as_float(c), __uint_as_float(u3)};
}
#define SB() __builtin_amdgcn_sched_barrier(0)
#define WGBAR()                                                     \
  do {                                                              \
    SB();                                                           \
    asm volatile("s_waitcnt lgkmcnt(0)\n\ts_barrier" ::: "memory"); \
    SB();                                                           \
  } while (0)
#define CBAR() asm volatile("" ::: "memory")
__device__ __forceinline__ void lds_signal(LAS unsigned* p) {
  if ((threadIdx.x & 63) == 0) asm volatile("ds_add_u32 %0, %1" ::"v"((unsigned)(uintptr_t)p), "v"(1u) : "memory");
  CBAR();
}
__device__ __forceinline__ unsigned lds_signal_ret(LAS unsigned* p) {
  unsigned old = 0;
  if ((threadIdx.x & 63) == 0)
    asm volatile("ds_add_rtn_u32 %0, %1, %2\n\ts_waitcnt lgkmcnt(0)" : "=&v"(old) : "v"((unsigned)(uintptr_t)p), "v"(1u) : "memory");
  old = __builtin_amdgcn_readfirstlane(old);
  CBAR();
  return old;
}
__device__ __forceinline__ void lds_wait(LAS unsigned* p, unsigned want) {
  SB();
  while (__builtin_amdgcn_readfirstlane(*(volatile LAS unsigned*)p) < want) __builtin_amdgcn_s_sleep(1);
  CBAR();
  SB();
}
__device__ __forceinline__ void lds_wait_fast(LAS unsigned* p, unsigned want) {
  SB();
  while (__builtin_amdgcn_readfirstlane(*(volatile LAS unsigned*)p) < want) {}
  CBAR();
  SB();
}
__device__ __forceinline__ void wait_until(unsigned long long t) {
  while (__builtin_amdgcn_s_memrealtime() < t) __builtin_amdgcn_s_sleep(1);
}
__device__ __forceinline__ f32x4 reduce16(float (&v)[16]) {
#pragma unroll
  for (int i = 0; i < 8; ++i) v[i] = swapsum32(v[i], v[i + 8]);
#pragma unroll
  for (int i = 0; i < 4; ++i) v[i] = swapsum16(v[i], v[i + 4]);
#pragma unroll
  for (int i = 0; i < 4; ++i) v[i] += dpp<D_ROR8>(v[i]);
  return f32x4{v[0], v[1], v[2], v[3]};
}


constexpr int PS0 = 36, PS1 = 16;
constexpr int K0 = 8;
constexpr int WGATE = 520;
constexpr int TGATE = 680;
constexpr int HGATE = 720;
constexpr int CODE_LINES = 120;
#define BAD4(a) ((((a)[0][1] ^ (a)[0][3]) ^ tgx) | (((a)[1][1] ^ (a)[1][3]) ^ tgx) | (((a)[2][1] ^ (a)[2][3]) ^ tgx) | (((a)[3][1] ^ (a)[3][3]) ^ tgx))
#define USUM4(a) ((unpack4((a)[0], tag) + unpack4((a)[1], tag)) + (unpack4((a)[2], tag) + unpack4((a)[3], tag)))

__global__ void __launch_bounds__(NTHR, 3)
fused_kernel(const u16* x, const u16* wq_, unsigned char* ws, const u16* gw, const u16* Kc, const u16* wg, const u16* wu,
             const u16* Vc, const u16* wo, const u16* wd, u16* out) {
  __shared__ __attribute__((aligned(16))) unsigned char lds_raw[LDS_BYTES];
  unsigned long long p_x, p_wq, p_ws, pc0;
  asm volatile("s_load_dwordx2 %0, %4, 0x0\n\ts_load_dwordx2 %1, %4, 0x8\n\ts_load_dwordx2 %2, %4, 0x10\n\ts_getpc_b64 %3"
               : "=&s"(p_x), "=&s"(p_wq), "=&s"(p_ws), "=&s"(pc0)
               : "s"(__builtin_amdgcn_kernarg_segment_ptr()));
  SB();
  LAS unsigned char* L = (LAS unsigned char*)lds_raw;
  LAS unsigned* cnt = (LAS unsigned*)(L + L_CNT);
  const int tid = threadIdx.x, lane = tid & 63;
  const int w = __builtin_amdgcn_readfirstlane(tid >> 6);
  const int bid = blockIdx.x, grp = bid & 7, r = bid >> 3;
  unsigned codew, epoch;
  v2u xq0, xq1;
  v4u big[4];
  {
    const int qrow = 128 * (r >> 3) + 32 * w + 4 * (lane >> 3);
    const unsigned qo = (qrow * HD + 512 * grp + 64 * (r & 7) + 8 * (lane & 7)) * 2;
    const int cl = 4 * r + (lane & 3);
    const unsigned coff = (cl < CODE_LINES ? cl : 0) * 128;
    asm volatile(
        "s_cmp_lg_u32 %[w], 9\n\ts_cbranch_scc1 1f\n\t"
        "global_load_dword %[cw], %[coff], %[pc]\n\t"
        "s_waitcnt lgkmcnt(0)\n\t"
        "global_load_dword %[ep], %[vz], %[pws] sc1\n"
        "1:\n\ts_cmp_gt_u32 %[w], 3\n\ts_cbranch_scc1 2f\n\t"
        "s_waitcnt lgkmcnt(0)\n\t"
        "global_load_dwordx4 %[b0], %[o0], %[pb] nt\n\t"
        "global_load_dwordx4 %[b1], %[o1], %[pb] nt\n\t"
        "global_load_dwordx4 %[b2], %[o2], %[pb] nt\n\t"
        "global_load_dwordx4 %[b3], %[o3], %[pb] nt\n\t"
        "global_load_dwordx2 %[xa], %[xoff], %[px]\n\t"
        "global_load_dwordx2 %[xb], %[xoff], %[px] offset:1024\n"
        "2:"
        : [cw] "=&v"(codew), [ep] "=&v"(epoch), [xa] "=&v"(xq0), [xb] "=&v"(xq1), [b0] "=&v"(big[0]), [b1] "=&v"(big[1]),
          [b2] "=&v"(big[2]), [b3] "=&v"(big[3])
        : [w] "s"(w), [coff] "v"(coff), [pc] "s"(pc0 & ~127ull), [xoff] "v"(qrow * 2), [px] "s"(p_x), [vz] "v"(0), [pws] "s"(p_ws),
          [pb] "s"(p_wq), [o0] "v"(qo), [o1] "v"(qo + HD * 2), [o2] "v"(qo + HD * 4), [o3] "v"(qo + HD * 6)
        : "memory", "scc");
  }
#define WGBAR_A()                                                   \
  do {                                                              \
    WGBAR();                                                        \
    asm volatile("" ::"s"(p_x), "s"(p_wq), "s"(p_ws));   \
  } while (0)
#define PRO_WAIT(N)                                             \
  do {                                                          \
    SB();                                                       \
    asm volatile("s_waitcnt vmcnt(" #N ")" ::: "memory");       \
    SB();                                                       \
  } while (0)
  const int sw = grp * 2 + (r >> 4);
  const size_t kvbase = ((size_t)((r >> 4) * 8 + grp) * SK + (SK - WINDOW)) * DH + (4 * (r & 15) + (w & 3)) * 512;
  const int gc = grp ^ 1;
  const int j = lane >> 4, n = lane & 15;
#define MAKE_RS() asm volatile("" : "+s"(ws)); const __amdgpu_buffer_rsrc_t rs = __builtin_amdgcn_make_buffer_rsrc(ws, 0, WS_END, 0x00020000)
  const f32x4 zero4 = {0.f, 0.f, 0.f, 0.f};
  constexpr float LOG2E = 1.4426950408889634f;

  if (w < 4) {
    WGBAR_A();
    PRO_WAIT(0);
    asm volatile("" : "+v"(xq0), "+v"(xq1), "+v"(big[0]), "+v"(big[1]), "+v"(big[2]), "+v"(big[3]));
    {
      float v[16];
#pragma unroll
      for (int e = 0; e < 8; ++e) {
        const unsigned p01 = permsel(big[1][e >> 1], big[0][e >> 1], e), p23 = permsel(big[3][e >> 1], big[2][e >> 1], e);
        v[e] = dot2(p23, xq0[1], dot2(p01, xq0[0], 0.f));
        v[8 + e] = dot2(p23, xq1[1], dot2(p01, xq1[0], 0.f));
      }
      const f32x4 a = reduce16(v);
      if ((lane & 8) == 0) *(LAS f32x4*)(L + L_QRED + ((w * 2 + (lane >> 5)) * 64 + 8 * (lane & 7) + 4 * ((lane >> 4) & 1)) * 4) = a;
    }
    lds_signal(cnt + 6);
    MAKE_RS();
    v4u vf[16];
    lds_wait(cnt + 7, 2);
#pragma unroll
    for (int i = 0; i < 16; ++i) vf[i] = ldnt(Vc + kvbase + (i ^ sw) * 32768 + lane * 8);
    SB();
    lds_signal(cnt + 9);
    v4u be[8];
#pragma unroll
    for (int q = 0; q < 4; ++q) {
#pragma unroll
      for (int e = 0; e < 8; ++e) be[e][q] = permsel(vf[2 * q + 1][e >> 1], vf[2 * q][e >> 1], e);
      SB();
    }
#pragma unroll
    for (int e = 0; e < 8; ++e) asm volatile("" : "+v"(be[e]));
    lds_wait(cnt + 5, 4);
    __builtin_amdgcn_s_setprio(3);
    const v4u pa0 = *(LAS v4u*)(L + L_P + (w * 2) * 1024 + lane * 16), pa1 = *(LAS v4u*)(L + L_P + (w * 2 + 1) * 1024 + lane * 16);
    const float Ls = sum4(*(LAS f32x4*)(L + L_LSUM + w * 16));
    const float Mh = max4(*(LAS f32x4*)(L + L_WMAX + w * 16));
    const unsigned tag = cnt[12];
    f32x4 oacc[8];
#pragma unroll
    for (int e = 0; e < 8; ++e) oacc[e] = mfma32(pa0, be[e], zero4);
    float inv = __builtin_amdgcn_rcpf(Ls);
    const unsigned lse = __float_as_uint(fmaf(Mh, LOG2E, __builtin_amdgcn_logf(Ls)));
    unsigned lA = lse ^ tag, lB = lse ^ tag_b(tag);
    int s2dst = lane < 32 ? S2_OFF + (grp * 32 + r) * 2048 + w * 512 + lane * 16 : OOB;
    int src = L_OP + (lane >> 5) * 2 * OP_W + (lane & 1) * 1152 + (w * 16 + ((lane & 31) >> 1)) * 16;
    asm volatile("" : "+v"(inv), "+v"(lA), "+v"(lB), "+v"(s2dst), "+v"(src));
    SB();
#pragma unroll
    for (int q = 0; q < 4; ++q) {
#pragma unroll
      for (int e = 0; e < 8; ++e) be[e][q] = permsel(vf[8 + 2 * q + 1][e >> 1], vf[8 + 2 * q][e >> 1], e);
      if (q < 3) SB();
    }
#pragma unroll
    for (int e = 0; e < 8; ++e) oacc[e] = mfma32(pa1, be[e], oacc[e]);
    *(LAS f32x4*)(L + L_OP + w * OP_W + lane * 16) = f32x4{oacc[0][0], oacc[1][0], oacc[2][0], oacc[3][0]};
    *(LAS f32x4*)(L + L_OP + w * OP_W + 1152 + lane * 16) = f32x4{oacc[4][0], oacc[5][0], oacc[6][0], oacc[7][0]};
    if (lds_signal_ret(cnt + 0) != 3u) lds_wait_fast(cnt + 0, 4);
    {
      const f32x4 t = *(LAS f32x4*)(L + src) + *(LAS f32x4*)(L + src + OP_W);
      const float a0 = swapsum32(t[0], t[0]), a1 = swapsum32(t[1], t[1]), a2 = swapsum32(t[2], t[2]), a3 = swapsum32(t[3], t[3]);
      st_sc1(rs, s2dst, v4u{cvtpk(a0 * inv, a1 * inv), lA, cvtpk(a2 * inv, a3 * inv), lB});
    }
    lds_signal(cnt + 10);
    return;
  }

  if (w < 8) {
    __builtin_amdgcn_s_setprio(2);
    const int k = w - 4;
    const unsigned long long t_gate = __builtin_amdgcn_s_memrealtime() + HGATE;
    const u16* kp = Kc + kvbase + lane * 8;
    WGBAR_A();
    v4u kf[16];
#pragma unroll
    for (int i = 0; i < K0; ++i) kf[i] = ldnt(kp + (i ^ sw) * 32768);
    SB();
    lds_wait(cnt + 8, 1);
#pragma unroll
    for (int i = K0; i < 16; ++i) kf[i] = ldnt(kp + (i ^ sw) * 32768);
    SB();
    LAS unsigned char* Kl = L + L_K + k * 16384;
#pragma unroll
    for (int i = 0; i < 16; ++i) {
      const int key = 4 * i + j;
      *(LAS v4u*)(Kl + key * 256 + ((n ^ (key & 15)) << 4)) = kf[i];
    }
    v4u ka[4][4];
    {
      const int kin = 4 * (n & 3) + (n >> 2);
#pragma unroll
      for (int t = 0; t < 4; ++t)
#pragma unroll
        for (int st = 0; st < 4; ++st) ka[t][st] = *(LAS v4u*)(Kl + (16 * t + kin) * 256 + (((4 * st + j) ^ kin) << 4));
    }
    lds_wait(cnt + 2, 2);
    v4u qf[4];
#pragma unroll
    for (int st = 0; st < 4; ++st) qf[st] = *(LAS v4u*)(L + L_Q + (n >> 2) * 256 + (4 * st + j) * 16);
    f32x4 sc[4];
#pragma unroll
    for (int t = 0; t < 4; ++t) sc[t] = zero4;
#pragma unroll
    for (int st = 0; st < 4; ++st)
#pragma unroll
      for (int t = 0; t < 4; ++t) sc[t] = mfma32(ka[t][st], qf[st], sc[t]);
#pragma unroll
    for (int t = 0; t < 4; ++t) {
      const unsigned a = cvtpk(sc[t][0], sc[t][1]), b = cvtpk(sc[t][2], sc[t][3]);
      const unsigned c = cvtpk(bflo(a) * 0.08838834764831845f, bfhi(a) * 0.08838834764831845f);
      const unsigned d = cvtpk(bflo(b) * 0.08838834764831845f, bfhi(b) * 0.08838834764831845f);
      sc[t] = f32x4{bflo(c), bfhi(c), bflo(d), bfhi(d)};
    }
    const float mx = xrow_max(fmaxf(fmaxf(max4(sc[0]), max4(sc[1])), fmaxf(max4(sc[2]), max4(sc[3]))));
    if (lane < 16 && (lane & 3) == 0) *(LAS float*)(L + L_WMAX + (lane + k) * 4) = mx;
    if (lds_signal_ret(cnt + 1) != 3u) lds_wait_fast(cnt + 1, 4);
    const float Mc = LOG2E * max4(*(LAS f32x4*)(L + L_WMAX + (n >> 2) * 16));
    v4u pa[2];
#pragma unroll
    for (int t = 0; t < 4; ++t) {
      float p[4];
#pragma unroll
      for (int i = 0; i < 4; ++i) p[i] = __builtin_amdgcn_exp2f(fmaf(sc[t][i], LOG2E, -Mc));
      pa[t >> 1][2 * (t & 1)] = cvtpk(p[0], p[1]);
      pa[t >> 1][2 * (t & 1) + 1] = cvtpk(p[2], p[3]);
    }
    *(LAS v4u*)(L + L_P + (k * 2) * 1024 + lane * 16) = pa[0];
    *(LAS v4u*)(L + L_P + (k * 2 + 1) * 1024 + lane * 16) = pa[1];
    const v4u ones = {0x3F803F80u, 0x3F803F80u, 0x3F803F80u, 0x3F803F80u};
    const f32x4 ls = mfma32(pa[1], ones, mfma32(pa[0], ones, zero4));
    if (n == 0) *(LAS float*)(L + L_LSUM + (j * 4 + k) * 4) = ls[0];
    lds_signal(cnt + 5);
    if (k == 0) {
      MAKE_RS();
      const unsigned tag = cnt[12], tgx = tag ^ tag_b(tag);
      const int cc = 224 * gc + 56 * (r >> 3) + 2 * ((lane & 31) < 28 ? (lane & 31) : 27);
      const int hoff = F1_OFF + ((lane >> 5) * 28 + (cc >> 6)) * 2048 + (cc & 63) * 8;
      const float omg = bfround(1.0f - __uint_as_float(cnt[13 + (lane >> 5)]));
      const u16* wdp = wd + (size_t)(224 * gc + 56 * (r >> 3) + 7 * (lane >> 3)) * DM + 64 * (r & 7) + 8 * (lane & 7);
      const int c0 = 2 * (lane & 31), c1 = c0 + 1;
      int hw0 = (lane & 31) < 28 ? L_H + (lane >> 5) * 128 + (c0 / 7) * 16 + (c0 % 7) * 2 : L_DUMP + lane * 4;
      int hw1 = (lane & 31) < 28 ? L_H + (lane >> 5) * 128 + (c1 / 7) * 16 + (c1 % 7) * 2 : L_DUMP + lane * 4 + 2;
      int har = L_H + (n >> 3) * 128 + (2 * j + ((n >> 2) & 1)) * 16;
      int redw = (j & 1) == (n >> 3) ? L_RED + ((8 + (j & 1)) * 2 + (j >> 1)) * 256 + (n & 7) * 32 : L_DUMP + 1024 + lane * 32;
      if (lane < 16) *(LAS u16*)(L + L_H + lane * 16 + 14) = 0;
      lds_wait(cnt + 10, 4);
      v4u dfr[7], db[8];
#pragma unroll
      for (int i = 0; i < 7; ++i) dfr[i] = ldnt(wdp + i * DM);
      SB();
      wait_until(t_gate);
      v4u hg[4];
      bool hh[4];
#pragma unroll
      for (int p = 0; p < 4; ++p) hg[p] = ld_sc1(rs, hoff + p * 512);
      SB();
#pragma unroll
      for (int e = 0; e < 8; ++e) {
        db[e][0] = permsel(dfr[1][e >> 1], dfr[0][e >> 1], e);
        db[e][1] = permsel(dfr[3][e >> 1], dfr[2][e >> 1], e);
        db[e][2] = permsel(dfr[5][e >> 1], dfr[4][e >> 1], e);
        db[e][3] = permsel(0u, dfr[6][e >> 1], e);
        asm volatile("" : "+v"(db[e]));
      }
      asm volatile("" : "+v"(hw0), "+v"(hw1), "+v"(har), "+v"(redw));
      SB();
#pragma unroll
      for (int p = 0; p < 4; ++p) hh[p] = (hg[p][1] ^ hg[p][3]) == tgx;
      bool done = __all(hh[0] & hh[1] & hh[2] & hh[3]);
#pragma nounroll
      for (unsigned spins = 0; !done && spins < SPIN_CAP; ++spins) {
        __builtin_amdgcn_s_sleep(2);
        CBAR();
        v4u t[4];
#pragma unroll
        for (int p = 0; p < 4; ++p) t[p] = ld_sc1(rs, hh[p] ? OOB : hoff + p * 512);
        SB();
#pragma unroll
        for (int p = 0; p < 4; ++p) {
          hg[p][0] = hh[p] ? hg[p][0] : t[p][0];
          hg[p][1] = hh[p] ? hg[p][1] : t[p][1];
          hg[p][2] = hh[p] ? hg[p][2] : t[p][2];
          hh[p] = hh[p] | ((t[p][1] ^ t[p][3]) == tgx);
        }
        done = __all(hh[0] & hh[1] & hh[2] & hh[3]);
      }
      const f32x4 hsum = USUM4(hg);
      float hv2[2];
#pragma unroll
      for (int c = 0; c < 2; ++c) {
        auto sres = __builtin_amdgcn_permlane32_swap(__float_as_uint(hsum[c]), __float_as_uint(hsum[2 + c]), false, false);
        const float gb = bfround(__uint_as_float(sres[0]));
        const float si = bfround(gb * __builtin_amdgcn_rcpf(1.0f + __expf(-gb)));
        hv2[c] = bfround(si * bfround(__uint_as_float(sres[1]))) * omg;
      }
      unsigned hp = cvtpk(hv2[0], hv2[1]);
      if (!done) hp = 0x7FC07FC0u;
      *(LAS u16*)(L + hw0) = (u16)hp;
      *(LAS u16*)(L + hw1) = (u16)(hp >> 16);
      CBAR();
      const v4u ha = *(LAS v4u*)(L + har);
      float res[8];
#pragma unroll
      for (int e = 0; e < 8; ++e) res[e] = mfma32(ha, db[e], zero4)[0];
      *(LAS f32x4*)(L + redw) = f32x4{res[0], res[1], res[2], res[3]};
      *(LAS f32x4*)(L + redw + 16) = f32x4{res[4], res[5], res[6], res[7]};
      WGBAR();
      return;
    }
    return;
  }

  {
    __builtin_amdgcn_s_setprio(3);
    const int tw = w - 8;
    MAKE_RS();
    const unsigned long long t_0 = __builtin_amdgcn_s_memrealtime();
    const int cblk = r & 7, hrel = r >> 3;
    const u16* wop = wo + (size_t)(512 * gc + 128 * hrel + 32 * tw + 4 * (lane >> 3)) * DM + 64 * cblk + 8 * (lane & 7);
    const u16* wdp = wd + (size_t)(224 * gc + 56 * hrel) * DM + 64 * cblk;
    int s2off = S2_OFF + (gc * 32 + (lane >> 3)) * 2048 + hrel * 512 + tw * 128 + (lane & 7) * 16;
    int s3off = S3_OFF + ((bid >> 2) * 32 + (lane >> 3)) * 512 + (bid & 3) * 128 + (lane & 7) * 16;
    asm volatile("" : "+v"(s2off), "+v"(s3off));
    v4u wfr[4];
#define TAGS_LDS()                                                                \
  do {                                                                            \
    tag = cnt[12];                                                                \
    tagB = tag_b(tag);                                                            \
    tgx = tag ^ tagB;                                                             \
  } while (0)
#define TAGS()                                                                    \
  do {                                                                            \
    asm volatile("" : "+v"(epoch));                                               \
    unsigned tg = epoch * 1664525u + 1013904223u;   \
    if (tg == 0u) tg = 1013904223u;                 \
    tag = tg;                                                                     \
    tagB = tag_b(tg);                                                             \
    tgx = tag ^ tagB;                                                             \
    asm volatile("" : "+v"(tag), "+v"(tagB), "+v"(tgx));                          \
  } while (0)
    unsigned tag, tagB, tgx;
    bool alive = true;
    const bool has_ft = r < 28;
    const int ft = grp * 28 + (r < 28 ? r : 0);
    const int fmat = ft >= 112, fcb = (ft - 112 * fmat) >> 2, frb = ft & 3;
    if (tw < 2) {
      if (tw == 1) {
        if (lane < 16) cnt[lane] = 0u;
        WGBAR_A();
        PRO_WAIT(0);
        asm volatile("" ::"v"(codew));
        TAGS();
        if (lane == 0) cnt[12] = tag;
        lds_wait(cnt + 6, 4);
        f32x4 a = *(LAS f32x4*)(L + L_QRED + (lane >> 5) * 1024 + (lane & 31) * 16) + *(LAS f32x4*)(L + L_QRED + (lane >> 5) * 1024 + 512 + (lane & 31) * 16);
        a = f32x4{swapsum32(a[0], a[0]), swapsum32(a[1], a[1]), swapsum32(a[2], a[2]), swapsum32(a[3], a[3])};
        st_sc1(rs, lane < 32 ? QG_OFF + ((grp * 8 + (r & 7)) * 4 + (r >> 3)) * 512 + lane * 16 : OOB, pack4(a, tag, tagB));
        lds_signal(cnt + 8);
      } else {
        const v4u gv = *(const GAS v4u*)(gw + 8 * lane);
        const v4u xa = *(const GAS v4u*)(x + 8 * lane), xb = *(const GAS v4u*)(x + DM + 8 * lane);
        WGBAR_A();
        float a0 = 0.f, a1 = 0.f;
#pragma unroll
        for (int q = 0; q < 4; ++q) {
          a0 += bfround(bflo(gv[q]) * bflo(xa[q])) + bfround(bfhi(gv[q]) * bfhi(xa[q]));
          a1 += bfround(bflo(gv[q]) * bflo(xb[q])) + bfround(bfhi(gv[q]) * bfhi(xb[q]));
        }
        float a = swapsum32(a0, a1);
        a = swapsum16(a, a);
        a += dpp<D_ROR8>(a);
        a += dpp<D_ROR4>(a);
        a += dpp<D_XOR2>(a);
        a += dpp<D_XOR1>(a);
        const float gt = bfround(__builtin_amdgcn_rcpf(1.0f + __expf(-bfround(a))));
        if ((lane & 31) == 0) cnt[13 + (lane >> 5)] = __float_as_uint(gt);
        lds_wait(cnt + 8, 1);
        TAGS_LDS();
      }
      const int qoff = QG_OFF + (grp * 8 + 4 * tw + (lane >> 4)) * 2048 + (r >> 4) * 256 + (lane & 15) * 16;
      v4u pq[2][4];
#pragma unroll
      for (int kk = 0; kk < 2; ++kk) {
        if (kk == 0) __builtin_amdgcn_s_sleep(PS0);
        else __builtin_amdgcn_s_sleep(PS1);
        CBAR();
#pragma unroll
        for (int p = 0; p < 4; ++p) pq[kk][p] = ld_sc1(rs, qoff + p * 512);
        SB();
        if (kk == 0) lds_signal(cnt + 7);
      }
      f32x4 qv;
      if (__all(BAD4(pq[0]) == 0u)) {
        qv = USUM4(pq[0]);
      } else {
        bool done = __all(BAD4(pq[1]) == 0u);
        unsigned spins = 0;
#pragma nounroll
        while (!done && ++spins < SPIN_CAP) {
          CBAR();
#pragma unroll
          for (int p = 0; p < 4; ++p) pq[1][p] = ld_sc1(rs, qoff + p * 512);
          done = __all(BAD4(pq[1]) == 0u);
        }
        alive = done;
        qv = USUM4(pq[1]);
      }
      *(LAS v2u*)(L + L_Q + tw * 512 + lane * 8) = v2u{cvtpk(qv[0], qv[1]), cvtpk(qv[2], qv[3])};
      lds_signal(cnt + 2);
      asm volatile("" ::"v"(pq[1][0]), "v"(pq[1][1]), "v"(pq[1][2]), "v"(pq[1][3]));
      lds_wait(cnt + 9, 4);
      pq[0][1] = ld_sc1(rs, bid < 32 ? s3off + tw * 8192 : OOB);
      pq[0][2] = ld_sc1(rs, bid < 32 ? s3off + tw * 8192 + 4096 : OOB);
      asm volatile("" ::"v"(pq[0][1]), "v"(pq[0][2]));
    } else {
      const int frow = 128 * frb + 64 * (tw & 1) + 8 * (lane >> 3);
      const v4u xa = *(const GAS v4u*)(x + frow), xb = *(const GAS v4u*)(x + DM + frow);
      const u16* wfp = (fmat ? wu : wg) + (size_t)frow * FF + 64 * fcb + 8 * (lane & 7);
      unsigned tl = 0;
      if (tw == 2 ? lane == 0 : ((lane & 7) == 0 && lane < 24))
        tl = __builtin_nontemporal_load((const GAS unsigned*)(tw == 2 ? Vc + kvbase : lane < 8 ? wfp : lane < 16 ? wop : wdp));
      WGBAR_A();
      asm volatile("" ::"v"(xa), "v"(xb), "v"(tl));
      lds_wait(cnt + 9, 4);
      TAGS_LDS();
      wait_until(t_0 + WGATE);
      v4u wf[8];
#pragma unroll
      for (int i = 0; i < 8; ++i) wf[i] = ldnt(wfp + (has_ft ? i * FF : 0));
      SB();
      float v[16];
#pragma unroll
      for (int e = 0; e < 16; ++e) v[e] = 0.f;
#pragma unroll
      for (int q = 0; q < 4; ++q) {
#pragma unroll
        for (int e = 0; e < 8; ++e) {
          const unsigned pk = permsel(wf[2 * q + 1][e >> 1], wf[2 * q][e >> 1], e);
          v[e] = dot2(pk, xa[q], v[e]);
          v[8 + e] = dot2(pk, xb[q], v[8 + e]);
        }
        SB();
      }
      const f32x4 fa = reduce16(v);
      if ((lane & 8) == 0) *(LAS f32x4*)(L + L_FRED + (((tw & 1) * 2 + (lane >> 5)) * 64 + 8 * (lane & 7) + 4 * ((lane >> 4) & 1)) * 4) = fa;
      const bool last = lds_signal_ret(cnt + 4) == 1u;
      const f32x2 a0 = *(LAS f32x2*)(L + L_FRED + (lane & 31) * 8) + *(LAS f32x2*)(L + L_FRED + 512 + (lane & 31) * 8);
      const f32x2 a1 = *(LAS f32x2*)(L + L_FRED + 256 + (lane & 31) * 8) + *(LAS f32x2*)(L + L_FRED + 768 + (lane & 31) * 8);
      asm volatile("buffer_store_dwordx4 %0, %1, %2, 0 offen sc1\n\ts_nop 1" ::"v"(pack4(f32x4{a0[0], a0[1], a1[0], a1[1]}, tag, tagB)),
                   "v"(last && has_ft && lane < 32 ? F1_OFF + ft * 512 + lane * 16 : OOB), "s"(rs) : "memory");
    }
    lds_wait(cnt + 10, 4);
#pragma unroll
    for (int i = 0; i < 4; ++i) wfr[i] = ldnt(wop + i * DM);
    SB();
    const float gate = __uint_as_float(cnt[13 + (lane >> 5)]);
    unsigned o0[4], o1[4], o2[4];
    const int rho = 2 * j + ((n >> 2) & 1);
    int oaw = L_OA + tw * 128 + (lane >> 5) * 64 + (lane & 7) * 8 + ((lane >> 4) & 1) * 4;
    int oar = L_OA + tw * 128 + (n >> 3) * 64 + rho * 8;
    int redw = (j & 1) == (n >> 3) ? L_RED + ((tw * 2 + (j & 1)) * 2 + (j >> 1)) * 256 + (n & 7) * 32 : L_DUMP + 4096 + lane * 32;
    v2u wb[8];
    {
      bool ho[4];
      v4u od[4];
      wait_until(t_0 + TGATE);
#pragma unroll
      for (int i = 0; i < 4; ++i) od[i] = ld_sc1(rs, s2off + i * 8 * 2048);
      SB();
#pragma unroll
      for (int e = 0; e < 8; ++e) {
        wb[e][0] = permsel(wfr[1][e >> 1], wfr[0][e >> 1], e);
        wb[e][1] = permsel(wfr[3][e >> 1], wfr[2][e >> 1], e);
        asm volatile("" : "+v"(wb[e]));
      }
      asm volatile("" : "+v"(oaw), "+v"(oar), "+v"(redw));
      SB();
#pragma unroll
      for (int i = 0; i < 4; ++i) {
        ho[i] = (od[i][1] ^ od[i][3]) == tgx;
        o0[i] = od[i][0]; o1[i] = od[i][1]; o2[i] = od[i][2];
      }
      unsigned spins = 0;
#pragma nounroll
      while (true) {
        const int miss = __builtin_popcountll(__ballot(!ho[0])) + __builtin_popcountll(__ballot(!ho[1])) +
                         __builtin_popcountll(__ballot(!ho[2])) + __builtin_popcountll(__ballot(!ho[3]));
        if (miss == 0) break;
        if (++spins >= SPIN_CAP) { alive = false; break; }
        if (miss > 128) __builtin_amdgcn_s_sleep(6);
        else if (miss > 40) __builtin_amdgcn_s_sleep(2);
        CBAR();
#pragma unroll
        for (int i = 0; i < 4; ++i) od[i] = ld_sc1(rs, ho[i] ? OOB : s2off + i * 8 * 2048);
        SB();
#pragma unroll
        for (int i = 0; i < 4; ++i) {
          o0[i] = ho[i] ? o0[i] : od[i][0];
          o1[i] = ho[i] ? o1[i] : od[i][1];
          o2[i] = ho[i] ? o2[i] : od[i][2];
          ho[i] = ho[i] | ((od[i][1] ^ od[i][3]) == tgx);
        }
      }
    }
    {
      float ls[4], ov[10];
#pragma unroll
      for (int i = 0; i < 4; ++i) ls[i] = __uint_as_float(o1[i] ^ tag);
      float mxb[2];
      {
        auto s0 = __builtin_amdgcn_permlane32_swap(__float_as_uint(fmaxf(ls[0], ls[1])), __float_as_uint(fmaxf(ls[2], ls[3])), false, false);
        float m = fmaxf(__uint_as_float(s0[0]), __uint_as_float(s0[1]));
        auto s1 = __builtin_amdgcn_permlane16_swap(__float_as_uint(m), __float_as_uint(m), false, false);
        m = fmaxf(__uint_as_float(s1[0]), __uint_as_float(s1[1]));
        m = fmaxf(m, dpp<D_ROR8>(m));
        auto s2 = __builtin_amdgcn_permlane32_swap(__float_as_uint(m), __float_as_uint(m), false, false);
        mxb[0] = __uint_as_float(s2[0]);
        mxb[1] = __uint_as_float(s2[1]);
      }
#pragma unroll
      for (int bb = 0; bb < 2; ++bb) {
        const float w0 = __builtin_amdgcn_exp2f(ls[2 * bb] - mxb[bb]), w1 = __builtin_amdgcn_exp2f(ls[2 * bb + 1] - mxb[bb]);
        ov[4 * bb + 0] = w0 * bflo(o0[2 * bb]) + w1 * bflo(o0[2 * bb + 1]);
        ov[4 * bb + 1] = w0 * bfhi(o0[2 * bb]) + w1 * bfhi(o0[2 * bb + 1]);
        ov[4 * bb + 2] = w0 * bflo(o2[2 * bb]) + w1 * bflo(o2[2 * bb + 1]);
        ov[4 * bb + 3] = w0 * bfhi(o2[2 * bb]) + w1 * bfhi(o2[2 * bb + 1]);
        ov[8 + bb] = w0 + w1;
      }
#pragma unroll
      for (int i = 0; i < 4; ++i) ov[i] = swapsum32(ov[i], ov[i + 4]);
      float W = swapsum32(ov[8], ov[9]);
      ov[0] = swapsum16(ov[0], ov[2]);
      ov[1] = swapsum16(ov[1], ov[3]);
      W = swapsum16(W, W);
      ov[0] += dpp<D_ROR8>(ov[0]);
      ov[1] += dpp<D_ROR8>(ov[1]);
      W += dpp<D_ROR8>(W);
      float sc = gate * __builtin_amdgcn_rcpf(W);
      if (!alive) sc = __uint_as_float(0x7FC00000u);
      *(LAS unsigned*)(L + oaw) = cvtpk(sc * ov[0], sc * ov[1]);
      CBAR();
      const v2u ao = *(LAS v2u*)(L + oar);
      float res[8];
#pragma unroll
      for (int e = 0; e < 8; ++e) res[e] = mfma16(ao, wb[e], zero4)[0];
      *(LAS f32x4*)(L + redw) = f32x4{res[0], res[1], res[2], res[3]};
      *(LAS f32x4*)(L + redw + 16) = f32x4{res[4], res[5], res[6], res[7]};
    }
    WGBAR();
    if (tw == 0) {
      f32x4 a = zero4;
#pragma unroll
      for (int t = 0; t < 5; ++t) a += *(LAS f32x4*)(L + L_RED + ((lane >> 5) * 5 + t) * 512 + (lane & 31) * 16);
      const f32x4 s = {swapsum32(a[0], a[0]), swapsum32(a[1], a[1]), swapsum32(a[2], a[2]), swapsum32(a[3], a[3])};
      if (lane < 32) st_sc1(rs, S3_OFF + (cblk * 32 + 4 * gc + hrel) * 512 + lane * 16, pack4(s, tag, tagB));
    } else if (tw == 1 && bid < 32) {
      v4u pd[4];
      unsigned spins = 0;
      bool done;
#pragma nounroll
      do {
        CBAR();
#pragma unroll
        for (int i = 0; i < 4; ++i) pd[i] = ld_sc1(rs, s3off + i * 4096);
        SB();
        done = __all(BAD4(pd) == 0u);
      } while (!done && ++spins < SPIN_CAP);
      alive = alive && done;
      const f32x4 a = USUM4(pd);
      float b0 = swapsum32(a[0], a[2]), b1 = swapsum32(a[1], a[3]);
      b0 = swapsum16(b0, b0);
      b1 = swapsum16(b1, b1);
      b0 += dpp<D_ROR8>(b0);
      b1 += dpp<D_ROR8>(b1);
      unsigned pk = cvtpk(b0, b1);
      if (!alive) pk = 0x7FC07FC0u;
      if ((lane & 24) == 0)
        __builtin_amdgcn_raw_buffer_store_b32(pk, __builtin_amdgcn_make_buffer_rsrc(out, 0, 2048, 0x00020000),
                                              (((bid >> 1) & 1) * DM + 64 * (bid >> 2) + 32 * (bid & 1) + 4 * (lane & 7) + 2 * (lane >> 5)) * 2, 0, 0);
      if (bid == 0 && lane == 0) __hip_atomic_store((GAS unsigned*)(ws + EPOCH_OFF), tag, RLX_AGENT);
    }
  }
}

}

extern "C" void kernel_launch(void* const* d_in, const int* in_sizes, int n_in, void* d_out, int out_size, void* d_ws,
                              size_t ws_size, hipStream_t stream) {
  using gm::u16;
  (void)in_sizes; (void)n_in; (void)out_size; (void)ws_size;
  gm::fused_kernel<<<gm::NWG, gm::NTHR, 0, stream>>>(
      (const u16*)d_in[0], (const u16*)d_in[3], (unsigned char*)d_ws, (const u16*)d_in[9], (const u16*)d_in[1],
      (const u16*)d_in[6], (const u16*)d_in[7], (const u16*)d_in[2], (const u16*)d_in[4], (const u16*)d_in[8], (u16*)d_out);
}
```

```cpp
#include <hip/hip_runtime.h>
#include <hip/hip_bf16.h>
#include <stdint.h>

namespace gm {

typedef unsigned v4u __attribute__((ext_vector_type(4)));
typedef unsigned v2u __attribute__((ext_vector_type(2)));
typedef float f32x4 __attribute__((ext_vector_type(4)));
typedef float f32x2 __attribute__((ext_vector_type(2)));
typedef __bf16 bf16x8_t __attribute__((ext_vector_type(8)));
typedef __bf16 bf16x2_t __attribute__((ext_vector_type(2)));
typedef unsigned short u16;

#define GAS __attribute__((address_space(1)))
#define LAS __attribute__((address_space(3)))
#define RLX_AGENT __ATOMIC_RELAXED, __HIP_MEMORY_SCOPE_AGENT

constexpr int SK = 32768, WINDOW = 4096, DH = 128, DM = 512, FF = 1792, HD = 4096;
constexpr int NWG = 256, NTHR = 768, NWAVES = 12;

constexpr int EPOCH_OFF = 0;
constexpr int QG_OFF = 4096;
constexpr int F1_OFF = QG_OFF + 256 * 512;
constexpr int S2_OFF = F1_OFF + 224 * 512;
constexpr int S3_OFF = S2_OFF + 8 * 32 * 2048;
constexpr int WS_END = S3_OFF + 8 * 32 * 512;

constexpr int L_CNT = 0;
constexpr int L_QRED = 64;
constexpr int L_FRED = L_QRED + 2048;
constexpr int L_Q = L_FRED + 1024;
constexpr int L_H = L_Q + 1024;
constexpr int L_OA = L_H + 256;
constexpr int L_WMAX = L_OA + 512;
constexpr int L_LSUM = L_WMAX + 64;
constexpr int L_RED = L_LSUM + 64;
constexpr int L_P = L_RED + 5120;
constexpr int L_OP = L_P + 8192;
constexpr int OP_W = 2304;
constexpr int L_K = L_OP + 4 * OP_W;
constexpr int L_DUMP = L_K;
constexpr int L_END = L_K + 4 * 16384;
constexpr int LDS_BYTES = 100 * 1024;
static_assert(L_END <= LDS_BYTES && (L_OP % 16) == 0 && (L_K % 16) == 0 && (L_RED % 16) == 0 && (L_P % 16) == 0, "lds map");

constexpr unsigned SPIN_CAP = 1u << 18;
constexpr int AUX_SC1 = 16;
constexpr int OOB = 0x7FFFFF00;

__device__ __forceinline__ v4u ldnt(const void* p) { return __builtin_nontemporal_load((const GAS v4u*)p); }
__device__ __forceinline__ float dot2(unsigned a, unsigned b, float c) {
  return __builtin_amdgcn_fdot2_f32_bf16(__builtin_bit_cast(bf16x2_t, a), __builtin_bit_cast(bf16x2_t, b), c, false);
}
__device__ __forceinline__ unsigned cvtpk(float lo, float hi) {
  bf16x2_t r = {(__bf16)lo, (__bf16)hi};
  return __builtin_bit_cast(unsigned, r);
}
__device__ __forceinline__ float bfround(float v) { return (float)(__bf16)v; }
__device__ __forceinline__ float bflo(unsigned u) { return __uint_as_float(u << 16); }
__device__ __forceinline__ float bfhi(unsigned u) { return __uint_as_float(u & 0xffff0000u); }
template <int CTRL> __device__ __forceinline__ float dpp(float x) {
  return __builtin_bit_cast(float, __builtin_amdgcn_mov_dpp(__builtin_bit_cast(int, x), CTRL, 0xf, 0xf, true));
}
constexpr int D_XOR1 = 0xB1, D_XOR2 = 0x4E, D_ROR8 = 0x128, D_ROR4 = 0x124;
__device__ __forceinline__ float swapsum32(float a, float b) {
  auto s = __builtin_amdgcn_permlane32_swap(__float_as_uint(a), __float_as_uint(b), false, false);
  return __uint_as_float(s[0]) + __uint_as_float(s[1]);
}
__device__ __forceinline__ float swapsum16(float a, float b) {
  auto s = __builtin_amdgcn_permlane16_swap(__float_as_uint(a), __float_as_uint(b), false, false);
  return __uint_as_float(s[0]) + __uint_as_float(s[1]);
}
__device__ __forceinline__ float xrow_max(float x) {
  auto s = __builtin_amdgcn_permlane16_swap(__float_as_uint(x), __float_as_uint(x), false, false);
  x = fmaxf(__uint_as_float(s[0]), __uint_as_float(s[1]));
  auto t = __builtin_amdgcn_permlane32_swap(__float_as_uint(x), __float_as_uint(x), false, false);
  return fmaxf(__uint_as_float(t[0]), __uint_as_float(t[1]));
}
__device__ __forceinline__ float max4(f32x4 a) { return fmaxf(fmaxf(a[0], a[1]), fmaxf(a[2], a[3])); }
__device__ __forceinline__ float sum4(f32x4 a) { return (a[0] + a[1]) + (a[2] + a[3]); }
__device__ __forceinline__ f32x4 mfma32(v4u a, v4u b, f32x4 c) {
  return __builtin_amdgcn_mfma_f32_16x16x32_bf16(__builtin_bit_cast(bf16x8_t, a), __builtin_bit_cast(bf16x8_t, b), c, 0, 0, 0);
}
typedef short s16x4 __attribute__((ext_vector_type(4)));
__device__ __forceinline__ f32x4 mfma16(v2u a, v2u b, f32x4 c) {
  return __builtin_amdgcn_mfma_f32_16x16x16bf16_1k(__builtin_bit_cast(s16x4, a), __builtin_bit_cast(s16x4, b), c, 0, 0, 0);
}
__device__ __forceinline__ unsigned permsel(unsigned hi, unsigned lo, int e) {
  return __builtin_amdgcn_perm(hi, lo, (e & 1) ? 0x07060302u : 0x05040100u);
}
__device__ __forceinline__ v4u ld_sc1(__amdgpu_buffer_rsrc_t rs, int off) {
  return __builtin_amdgcn_raw_buffer_load_b128(rs, off, 0, AUX_SC1);
}
__device__ __forceinline__ void st_sc1(__amdgpu_buffer_rsrc_t rs, int off, v4u v) {
  __builtin_amdgcn_raw_buffer_store_b128(v, rs, off, 0, AUX_SC1);
}
__device__ __forceinline__ unsigned tag_b(unsigned t) { return (t << 1) ^ ((t >> 31) ? 0x04C11DB7u : 0u); }
__device__ __forceinline__ v4u pack4(f32x4 v, unsigned tA, unsigned tB) {
  const unsigned u3 = __float_as_uint(v[3]);
  const unsigned a = __builtin_amdgcn_perm(__float_as_uint(v[0]), u3, 0x07060503u);
  const unsigned b = __builtin_amdgcn_perm(__float_as_uint(v[1]), u3, 0x07060502u);
  const unsigned c = __builtin_amdgcn_perm(__float_as_uint(v[2]), u3, 0x07060501u);
  return v4u{a, c ^ tA, b, c ^ tB};
}
__device__ __forceinline__ f32x4 unpack4(v4u p, unsigned tA) {
  const unsigned c = p[1] ^ tA;
  const unsigned t = __builtin_amdgcn_perm(p[0], p[2], 0x04000c0cu);
  const unsigned u3 = __builtin_amdgcn_perm(t, c, 0x0706000cu);
  return f32x4{__uint_as_float(p[0]), __uint_as_float(p[2]), __uint_as_float(c), __uint_as_float(u3)};
}
#define SB() __builtin_amdgcn_sched_barrier(0)
#define WGBAR()                                                     \
  do {                                                              \
    SB();                                                           \
    asm volatile("s_waitcnt lgkmcnt(0)\n\ts_barrier" ::: "memory"); \
    SB();                                                           \
  } while (0)
#define CBAR() asm volatile("" ::: "memory")
__device__ __forceinline__ void lds_signal(LAS unsigned* p) {
  if ((threadIdx.x & 63) == 0) asm volatile("ds_add_u32 %0, %1" ::"v"((unsigned)(uintptr_t)p), "v"(1u) : "memory");
  CBAR();
}
__device__ __forceinline__ unsigned lds_signal_ret(LAS unsigned* p) {
  unsigned old = 0;
  if ((threadIdx.x & 63) == 0)
    asm volatile("ds_add_rtn_u32 %0, %1, %2\n\ts_waitcnt lgkmcnt(0)" : "=&v"(old) : "v"((unsigned)(uintptr_t)p), "v"(1u) : "memory");
  old = __builtin_amdgcn_readfirstlane(old);
  CBAR();
  return old;
}
__device__ __forceinline__ void lds_wait(LAS unsigned* p, unsigned want) {
  SB();
  while (__builtin_amdgcn_readfirstlane(*(volatile LAS unsigned*)p) < want) __builtin_amdgcn_s_sleep(1);
  CBAR();
  SB();
}
__device__ __forceinline__ void lds_wait_fast(LAS unsigned* p, unsigned want) {
  SB();
  while (__builtin_amdgcn_readfirstlane(*(volatile LAS unsigned*)p) < want) {}
  CBAR();
  SB();
}
__device__ __forceinline__ void wait_until(unsigned long long t) {
  while (__builtin_amdgcn_s_memrealtime() < t) __builtin_amdgcn_s_sleep(1);
}
__device__ __forceinline__ f32x4 reduce16(float (&v)[16]) {
#pragma unroll
  for (int i = 0; i < 8; ++i) v[i] = swapsum32(v[i], v[i + 8]);
#pragma unroll
  for (int i = 0; i < 4; ++i) v[i] = swapsum16(v[i], v[i + 4]);
#pragma unroll
  for (int i = 0; i < 4; ++i) v[i] += dpp<D_ROR8>(v[i]);
  return f32x4{v[0], v[1], v[2], v[3]};
}


constexpr int PS0 = 42, PS1 = 16;
constexpr int K0 = 8;
constexpr int WGATE = 540;
constexpr int TGATE = 710;
constexpr int HGATE = 750;
constexpr int CODE_LINES = 120;
#define BAD4(a) ((((a)[0][1] ^ (a)[0][3]) ^ tgx) | (((a)[1][1] ^ (a)[1][3]) ^ tgx) | (((a)[2][1] ^ (a)[2][3]) ^ tgx) | (((a)[3][1] ^ (a)[3][3]) ^ tgx))
#define USUM4(a) ((unpack4((a)[0], tag) + unpack4((a)[1], tag)) + (unpack4((a)[2], tag) + unpack4((a)[3], tag)))

__global__ void __launch_bounds__(NTHR, 3)
fused_kernel(const u16* x, const u16* wq_, unsigned char* ws, const u16* gw, const u16* Kc, const u16* wg, const u16* wu,
             const u16* Vc, const u16* wo, const u16* wd, u16* out) {
  __shared__ __attribute__((aligned(16))) unsigned char lds_raw[LDS_BYTES];
  unsigned long long p_x, p_wq, p_ws, pc0;
  asm volatile("s_load_dwordx2 %0, %4, 0x0\n\ts_load_dwordx2 %1, %4, 0x8\n\ts_load_dwordx2 %2, %4, 0x10\n\ts_getpc_b64 %3"
               : "=&s"(p_x), "=&s"(p_wq), "=&s"(p_ws), "=&s"(pc0)
               : "s"(__builtin_amdgcn_kernarg_segment_ptr()));
  SB();
  LAS unsigned char* L = (LAS unsigned char*)lds_raw;
  LAS unsigned* cnt = (LAS unsigned*)(L + L_CNT);
  const int tid = threadIdx.x, lane = tid & 63;
  const int w = __builtin_amdgcn_readfirstlane(tid >> 6);
  const int bid = blockIdx.x, grp = bid & 7, r = bid >> 3;
  unsigned codew, epoch;
  v2u xq0, xq1;
  v4u big[4];
  {
    const int qrow = 128 * (r >> 3) + 32 * w + 4 * (lane >> 3);
    const unsigned qo = (qrow * HD + 512 * grp + 64 * (r & 7) + 8 * (lane & 7)) * 2;
    const int cl = 4 * r + (lane & 3);
    const unsigned coff = (cl < CODE_LINES ? cl : 0) * 128;
    asm volatile(
        "s_cmp_lg_u32 %[w], 9\n\ts_cbranch_scc1 1f\n\t"
        "global_load_dword %[cw], %[coff], %[pc]\n\t"
        "s_waitcnt lgkmcnt(0)\n\t"
        "global_load_dword %[ep], %[vz], %[pws] sc1\n"
        "1:\n\ts_cmp_gt_u32 %[w], 3\n\ts_cbranch_scc1 2f\n\t"
        "s_waitcnt lgkmcnt(0)\n\t"
        "global_load_dwordx4 %[b0], %[o0], %[pb] nt\n\t"
        "global_load_dwordx4 %[b1], %[o1], %[pb] nt\n\t"
        "global_load_dwordx4 %[b2], %[o2], %[pb] nt\n\t"
        "global_load_dwordx4 %[b3], %[o3], %[pb] nt\n\t"
        "global_load_dwordx2 %[xa], %[xoff], %[px]\n\t"
        "global_load_dwordx2 %[xb], %[xoff], %[px] offset:1024\n"
        "2:"
        : [cw] "=&v"(codew), [ep] "=&v"(epoch), [xa] "=&v"(xq0), [xb] "=&v"(xq1), [b0] "=&v"(big[0]), [b1] "=&v"(big[1]),
          [b2] "=&v"(big[2]), [b3] "=&v"(big[3])
        : [w] "s"(w), [coff] "v"(coff), [pc] "s"(pc0 & ~127ull), [xoff] "v"(qrow * 2), [px] "s"(p_x), [vz] "v"(0), [pws] "s"(p_ws),
          [pb] "s"(p_wq), [o0] "v"(qo), [o1] "v"(qo + HD * 2), [o2] "v"(qo + HD * 4), [o3] "v"(qo + HD * 6)
        : "memory", "scc");
  }
#define WGBAR_A()                                                   \
  do {                                                              \
    WGBAR();                                                        \
    asm volatile("" ::"s"(p_x), "s"(p_wq), "s"(p_ws));   \
  } while (0)
#define PRO_WAIT(N)                                             \
  do {                                                          \
    SB();                                                       \
    asm volatile("s_waitcnt vmcnt(" #N ")" ::: "memory");       \
    SB();                                                       \
  } while (0)
  const int sw = grp * 2 + (r >> 4);
  const size_t kvbase = ((size_t)((r >> 4) * 8 + grp) * SK + (SK - WINDOW)) * DH + (4 * (r & 15) + (w & 3)) * 512;
  const int gc = grp ^ 1;
  const int j = lane >> 4, n = lane & 15;
#define MAKE_RS() asm volatile("" : "+s"(ws)); const __amdgpu_buffer_rsrc_t rs = __builtin_amdgcn_make_buffer_rsrc(ws, 0, WS_END, 0x00020000)
  const f32x4 zero4 = {0.f, 0.f, 0.f, 0.f};
  constexpr float LOG2E = 1.4426950408889634f;

  if (w < 4) {
    WGBAR_A();
    PRO_WAIT(0);
    asm volatile("" : "+v"(xq0), "+v"(xq1), "+v"(big[0]), "+v"(big[1]), "+v"(big[2]), "+v"(big[3]));
    {
      float v[16];
#pragma unroll
      for (int e = 0; e < 8; ++e) {
        const unsigned p01 = permsel(big[1][e >> 1], big[0][e >> 1], e), p23 = permsel(big[3][e >> 1], big[2][e >> 1], e);
        v[e] = dot2(p23, xq0[1], dot2(p01, xq0[0], 0.f));
        v[8 + e] = dot2(p23, xq1[1], dot2(p01, xq1[0], 0.f));
      }
      const f32x4 a = reduce16(v);
      if ((lane & 8) == 0) *(LAS f32x4*)(L + L_QRED + ((w * 2 + (lane >> 5)) * 64 + 8 * (lane & 7) + 4 * ((lane >> 4) & 1)) * 4) = a;
    }
    lds_signal(cnt + 6);
    MAKE_RS();
    v4u vf[16];
    lds_wait(cnt + 7, 2);
#pragma unroll
    for (int i = 0; i < 16; ++i) vf[i] = ldnt(Vc + kvbase + (i ^ sw) * 32768 + lane * 8);
    SB();
    lds_signal(cnt + 9);
    v4u be[8];
#pragma unroll
    for (int q = 0; q < 4; ++q) {
#pragma unroll
      for (int e = 0; e < 8; ++e) be[e][q] = permsel(vf[2 * q + 1][e >> 1], vf[2 * q][e >> 1], e);
      SB();
    }
#pragma unroll
    for (int e = 0; e < 8; ++e) asm volatile("" : "+v"(be[e]));
    lds_wait(cnt + 5, 4);
    __builtin_amdgcn_s_setprio(3);
    const v4u pa0 = *(LAS v4u*)(L + L_P + (w * 2) * 1024 + lane * 16), pa1 = *(LAS v4u*)(L + L_P + (w * 2 + 1) * 1024 + lane * 16);
    const float Ls = sum4(*(LAS f32x4*)(L + L_LSUM + w * 16));
    const float Mh = max4(*(LAS f32x4*)(L + L_WMAX + w * 16));
    const unsigned tag = cnt[12];
    f32x4 oacc[8];
#pragma unroll
    for (int e = 0; e < 8; ++e) oacc[e] = mfma32(pa0, be[e], zero4);
    float inv = __builtin_amdgcn_rcpf(Ls);
    const unsigned lse = __float_as_uint(fmaf(Mh, LOG2E, __builtin_amdgcn_logf(Ls)));
    unsigned lA = lse ^ tag, lB = lse ^ tag_b(tag);
    int s2dst = lane < 32 ? S2_OFF + (grp * 32 + r) * 2048 + w * 512 + lane * 16 : OOB;
    int src = L_OP + (lane >> 5) * 2 * OP_W + (lane & 1) * 1152 + (w * 16 + ((lane & 31) >> 1)) * 16;
    asm volatile("" : "+v"(inv), "+v"(lA), "+v"(lB), "+v"(s2dst), "+v"(src));
    SB();
#pragma unroll
    for (int q = 0; q < 4; ++q) {
#pragma unroll
      for (int e = 0; e < 8; ++e) be[e][q] = permsel(vf[8 + 2 * q + 1][e >> 1], vf[8 + 2 * q][e >> 1], e);
      if (q < 3) SB();
    }
#pragma unroll
    for (int e = 0; e < 8; ++e) oacc[e] = mfma32(pa1, be[e], oacc[e]);
    *(LAS f32x4*)(L + L_OP + w * OP_W + lane * 16) = f32x4{oacc[0][0], oacc[1][0], oacc[2][0], oacc[3][0]};
    *(LAS f32x4*)(L + L_OP + w * OP_W + 1152 + lane * 16) = f32x4{oacc[4][0], oacc[5][0], oacc[6][0], oacc[7][0]};
    if (lds_signal_ret(cnt + 0) != 3u) lds_wait_fast(cnt + 0, 4);
    {
      const f32x4 t = *(LAS f32x4*)(L + src) + *(LAS f32x4*)(L + src + OP_W);
      const float a0 = swapsum32(t[0], t[0]), a1 = swapsum32(t[1], t[1]), a2 = swapsum32(t[2], t[2]), a3 = swapsum32(t[3], t[3]);
      st_sc1(rs, s2dst, v4u{cvtpk(a0 * inv, a1 * inv), lA, cvtpk(a2 * inv, a3 * inv), lB});
    }
    lds_signal(cnt + 10);
    return;
  }

  if (w < 8) {
    __builtin_amdgcn_s_setprio(2);
    const int k = w - 4;
    const unsigned long long t_gate = __builtin_amdgcn_s_memrealtime() + HGATE;
    const u16* kp = Kc + kvbase + lane * 8;
    WGBAR_A();
    v4u kf[16];
#pragma unroll
    for (int i = 0; i < K0; ++i) kf[i] = ldnt(kp + (i ^ sw) * 32768);
    SB();
    lds_wait(cnt + 8, 1);
#pragma unroll
    for (int i = K0; i < 16; ++i) kf[i] = ldnt(kp + (i ^ sw) * 32768);
    SB();
    LAS unsigned char* Kl = L + L_K + k * 16384;
#pragma unroll
    for (int i = 0; i < 16; ++i) {
      const int key = 4 * i + j;
      *(LAS v4u*)(Kl + key * 256 + ((n ^ (key & 15)) << 4)) = kf[i];
    }
    v4u ka[4][4];
    {
      const int kin = 4 * (n & 3) + (n >> 2);
#pragma unroll
      for (int t = 0; t < 4; ++t)
#pragma unroll
        for (int st = 0; st < 4; ++st) ka[t][st] = *(LAS v4u*)(Kl + (16 * t + kin) * 256 + (((4 * st + j) ^ kin) << 4));
    }
    lds_wait(cnt + 2, 2);
    v4u qf[4];
#pragma unroll
    for (int st = 0; st < 4; ++st) qf[st] = *(LAS v4u*)(L + L_Q + (n >> 2) * 256 + (4 * st + j) * 16);
    f32x4 sc[4];
#pragma unroll
    for (int t = 0; t < 4; ++t) sc[t] = zero4;
#pragma unroll
    for (int st = 0; st < 4; ++st)
#pragma unroll
      for (int t = 0; t < 4; ++t) sc[t] = mfma32(ka[t][st], qf[st], sc[t]);
#pragma unroll
    for (int t = 0; t < 4; ++t) {
      const unsigned a = cvtpk(sc[t][0], sc[t][1]), b = cvtpk(sc[t][2], sc[t][3]);
      const unsigned c = cvtpk(bflo(a) * 0.08838834764831845f, bfhi(a) * 0.08838834764831845f);
      const unsigned d = cvtpk(bflo(b) * 0.08838834764831845f, bfhi(b) * 0.08838834764831845f);
      sc[t] = f32x4{bflo(c), bfhi(c), bflo(d), bfhi(d)};
    }
    const float mx = xrow_max(fmaxf(fmaxf(max4(sc[0]), max4(sc[1])), fmaxf(max4(sc[2]), max4(sc[3]))));
    if (lane < 16 && (lane & 3) == 0) *(LAS float*)(L + L_WMAX + (lane + k) * 4) = mx;
    if (lds_signal_ret(cnt + 1) != 3u) lds_wait_fast(cnt + 1, 4);
    const float Mc = LOG2E * max4(*(LAS f32x4*)(L + L_WMAX + (n >> 2) * 16));
    v4u pa[2];
#pragma unroll
    for (int t = 0; t < 4; ++t) {
      float p[4];
#pragma unroll
      for (int i = 0; i < 4; ++i) p[i] = __builtin_amdgcn_exp2f(fmaf(sc[t][i], LOG2E, -Mc));
      pa[t >> 1][2 * (t & 1)] = cvtpk(p[0], p[1]);
      pa[t >> 1][2 * (t & 1) + 1] = cvtpk(p[2], p[3]);
    }
    *(LAS v4u*)(L + L_P + (k * 2) * 1024 + lane * 16) = pa[0];
    *(LAS v4u*)(L + L_P + (k * 2 + 1) * 1024 + lane * 16) = pa[1];
    const v4u ones = {0x3F803F80u, 0x3F803F80u, 0x3F803F80u, 0x3F803F80u};
    const f32x4 ls = mfma32(pa[1], ones, mfma32(pa[0], ones, zero4));
    if (n == 0) *(LAS float*)(L + L_LSUM + (j * 4 + k) * 4) = ls[0];
    lds_signal(cnt + 5);
    if (k == 0) {
      MAKE_RS();
      const unsigned tag = cnt[12], tgx = tag ^ tag_b(tag);
      const int cc = 224 * gc + 56 * (r >> 3) + 2 * ((lane & 31) < 28 ? (lane & 31) : 27);
      const int hoff = F1_OFF + ((lane >> 5) * 28 + (cc >> 6)) * 2048 + (cc & 63) * 8;
      const float omg = bfround(1.0f - __uint_as_float(cnt[13 + (lane >> 5)]));
      const u16* wdp = wd + (size_t)(224 * gc + 56 * (r >> 3) + 7 * (lane >> 3)) * DM + 64 * (r & 7) + 8 * (lane & 7);
      const int c0 = 2 * (lane & 31), c1 = c0 + 1;
      int hw0 = (lane & 31) < 28 ? L_H + (lane >> 5) * 128 + (c0 / 7) * 16 + (c0 % 7) * 2 : L_DUMP + lane * 4;
      int hw1 = (lane & 31) < 28 ? L_H + (lane >> 5) * 128 + (c1 / 7) * 16 + (c1 % 7) * 2 : L_DUMP + lane * 4 + 2;
      int har = L_H + (n >> 3) * 128 + (2 * j + ((n >> 2) & 1)) * 16;
      int redw = (j & 1) == (n >> 3) ? L_RED + ((8 + (j & 1)) * 2 + (j >> 1)) * 256 + (n & 7) * 32 : L_DUMP + 1024 + lane * 32;
      if (lane < 16) *(LAS u16*)(L + L_H + lane * 16 + 14) = 0;
      lds_wait(cnt + 10, 4);
      v4u dfr[7], db[8];
#pragma unroll
      for (int i = 0; i < 7; ++i) dfr[i] = ldnt(wdp + i * DM);
      SB();
      wait_until(t_gate);
      v4u hg[4];
      bool hh[4];
#pragma unroll
      for (int p = 0; p < 4; ++p) hg[p] = ld_sc1(rs, hoff + p * 512);
      SB();
#pragma unroll
      for (int e = 0; e < 8; ++e) {
        db[e][0] = permsel(dfr[1][e >> 1], dfr[0][e >> 1], e);
        db[e][1] = permsel(dfr[3][e >> 1], dfr[2][e >> 1], e);
        db[e][2] = permsel(dfr[5][e >> 1], dfr[4][e >> 1], e);
        db[e][3] = permsel(0u, dfr[6][e >> 1], e);
        asm volatile("" : "+v"(db[e]));
      }
      asm volatile("" : "+v"(hw0), "+v"(hw1), "+v"(har), "+v"(redw));
      SB();
#pragma unroll
      for (int p = 0; p < 4; ++p) hh[p] = (hg[p][1] ^ hg[p][3]) == tgx;
      bool done = __all(hh[0] & hh[1] & hh[2] & hh[3]);
#pragma nounroll
      for (unsigned spins = 0; !done && spins < SPIN_CAP; ++spins) {
        __builtin_amdgcn_s_sleep(2);
        CBAR();
        v4u t[4];
#pragma unroll
        for (int p = 0; p < 4; ++p) t[p] = ld_sc1(rs, hh[p] ? OOB : hoff + p * 512);
        SB();
#pragma unroll
        for (int p = 0; p < 4; ++p) {
          hg[p][0] = hh[p] ? hg[p][0] : t[p][0];
          hg[p][1] = hh[p] ? hg[p][1] : t[p][1];
          hg[p][2] = hh[p] ? hg[p][2] : t[p][2];
          hh[p] = hh[p] | ((t[p][1] ^ t[p][3]) == tgx);
        }
        done = __all(hh[0] & hh[1] & hh[2] & hh[3]);
      }
      const f32x4 hsum = USUM4(hg);
      float hv2[2];
#pragma unroll
      for (int c = 0; c < 2; ++c) {
        auto sres = __builtin_amdgcn_permlane32_swap(__float_as_uint(hsum[c]), __float_as_uint(hsum[2 + c]), false, false);
        const float gb = bfround(__uint_as_float(sres[0]));
        const float si = bfround(gb * __builtin_amdgcn_rcpf(1.0f + __expf(-gb)));
        hv2[c] = bfround(si * bfround(__uint_as_float(sres[1]))) * omg;
      }
      unsigned hp = cvtpk(hv2[0], hv2[1]);
      if (!done) hp = 0x7FC07FC0u;
      *(LAS u16*)(L + hw0) = (u16)hp;
      *(LAS u16*)(L + hw1) = (u16)(hp >> 16);
      CBAR();
      const v4u ha = *(LAS v4u*)(L + har);
      float res[8];
#pragma unroll
      for (int e = 0; e < 8; ++e) res[e] = mfma32(ha, db[e], zero4)[0];
      *(LAS f32x4*)(L + redw) = f32x4{res[0], res[1], res[2], res[3]};
      *(LAS f32x4*)(L + redw + 16) = f32x4{res[4], res[5], res[6], res[7]};
      WGBAR();
      return;
    }
    return;
  }

  {
    __builtin_amdgcn_s_setprio(3);
    const int tw = w - 8;
    MAKE_RS();
    const unsigned long long t_0 = __builtin_amdgcn_s_memrealtime();
    const int cblk = r & 7, hrel = r >> 3;
    const u16* wop = wo + (size_t)(512 * gc + 128 * hrel + 32 * tw + 4 * (lane >> 3)) * DM + 64 * cblk + 8 * (lane & 7);
    const u16* wdp = wd + (size_t)(224 * gc + 56 * hrel) * DM + 64 * cblk;
    int s2off = S2_OFF + (gc * 32 + (lane >> 3)) * 2048 + hrel * 512 + tw * 128 + (lane & 7) * 16;
    int s3off = S3_OFF + ((bid >> 2) * 32 + (lane >> 3)) * 512 + (bid & 3) * 128 + (lane & 7) * 16;
    asm volatile("" : "+v"(s2off), "+v"(s3off));
    v4u wfr[4];
#define TAGS_LDS()                                                                \
  do {                                                                            \
    tag = cnt[12];                                                                \
    tagB = tag_b(tag);                                                            \
    tgx = tag ^ tagB;                                                             \
  } while (0)
#define TAGS()                                                                    \
  do {                                                                            \
    asm volatile("" : "+v"(epoch));                                               \
    unsigned tg = epoch * 1664525u + 1013904223u;   \
    if (tg == 0u) tg = 1013904223u;                 \
    tag = tg;                                                                     \
    tagB = tag_b(tg);                                                             \
    tgx = tag ^ tagB;                                                             \
    asm volatile("" : "+v"(tag), "+v"(tagB), "+v"(tgx));                          \
  } while (0)
    unsigned tag, tagB, tgx;
    bool alive = true;
    const bool has_ft = r < 28;
    const int ft = grp * 28 + (r < 28 ? r : 0);
    const int fmat = ft >= 112, fcb = (ft - 112 * fmat) >> 2, frb = ft & 3;
    if (tw < 2) {
      if (tw == 1) {
        if (lane < 16) cnt[lane] = 0u;
        WGBAR_A();
        PRO_WAIT(0);
        asm volatile("" ::"v"(codew));
        TAGS();
        if (lane == 0) cnt[12] = tag;
        lds_wait(cnt + 6, 4);
        f32x4 a = *(LAS f32x4*)(L + L_QRED + (lane >> 5) * 1024 + (lane & 31) * 16) + *(LAS f32x4*)(L + L_QRED + (lane >> 5) * 1024 + 512 + (lane & 31) * 16);
        a = f32x4{swapsum32(a[0], a[0]), swapsum32(a[1], a[1]), swapsum32(a[2], a[2]), swapsum32(a[3], a[3])};
        st_sc1(rs, lane < 32 ? QG_OFF + ((grp * 8 + (r & 7)) * 4 + (r >> 3)) * 512 + lane * 16 : OOB, pack4(a, tag, tagB));
        lds_signal(cnt + 8);
      } else {
        const v4u gv = *(const GAS v4u*)(gw + 8 * lane);
        const v4u xa = *(const GAS v4u*)(x + 8 * lane), xb = *(const GAS v4u*)(x + DM + 8 * lane);
        WGBAR_A();
        float a0 = 0.f, a1 = 0.f;
#pragma unroll
        for (int q = 0; q < 4; ++q) {
          a0 += bfround(bflo(gv[q]) * bflo(xa[q])) + bfround(bfhi(gv[q]) * bfhi(xa[q]));
          a1 += bfround(bflo(gv[q]) * bflo(xb[q])) + bfround(bfhi(gv[q]) * bfhi(xb[q]));
        }
        float a = swapsum32(a0, a1);
        a = swapsum16(a, a);
        a += dpp<D_ROR8>(a);
        a += dpp<D_ROR4>(a);
        a += dpp<D_XOR2>(a);
        a += dpp<D_XOR1>(a);
        const float gt = bfround(__builtin_amdgcn_rcpf(1.0f + __expf(-bfround(a))));
        if ((lane & 31) == 0) cnt[13 + (lane >> 5)] = __float_as_uint(gt);
        lds_wait(cnt + 8, 1);
        TAGS_LDS();
      }
      const int qoff = QG_OFF + (grp * 8 + 4 * tw + (lane >> 4)) * 2048 + (r >> 4) * 256 + (lane & 15) * 16;
      v4u pq[2][4];
#pragma unroll
      for (int kk = 0; kk < 2; ++kk) {
        if (kk == 0) __builtin_amdgcn_s_sleep(PS0);
        else __builtin_amdgcn_s_sleep(PS1);
        CBAR();
#pragma unroll
        for (int p = 0; p < 4; ++p) pq[kk][p] = ld_sc1(rs, qoff + p * 512);
        SB();
        if (kk == 0) lds_signal(cnt + 7);
      }
      f32x4 qv;
      if (__all(BAD4(pq[0]) == 0u)) {
        qv = USUM4(pq[0]);
      } else {
        bool done = __all(BAD4(pq[1]) == 0u);
        unsigned spins = 0;
#pragma nounroll
        while (!done && ++spins < SPIN_CAP) {
          CBAR();
#pragma unroll
          for (int p = 0; p < 4; ++p) pq[1][p] = ld_sc1(rs, qoff + p * 512);
          done = __all(BAD4(pq[1]) == 0u);
        }
        alive = done;
        qv = USUM4(pq[1]);
      }
      *(LAS v2u*)(L + L_Q + tw * 512 + lane * 8) = v2u{cvtpk(qv[0], qv[1]), cvtpk(qv[2], qv[3])};
      lds_signal(cnt + 2);
      asm volatile("" ::"v"(pq[1][0]), "v"(pq[1][1]), "v"(pq[1][2]), "v"(pq[1][3]));
      lds_wait(cnt + 9, 4);
      pq[0][1] = ld_sc1(rs, bid < 32 ? s3off + tw * 8192 : OOB);
      pq[0][2] = ld_sc1(rs, bid < 32 ? s3off + tw * 8192 + 4096 : OOB);
      asm volatile("" ::"v"(pq[0][1]), "v"(pq[0][2]));
    } else {
      const int frow = 128 * frb + 64 * (tw & 1) + 8 * (lane >> 3);
      const v4u xa = *(const GAS v4u*)(x + frow), xb = *(const GAS v4u*)(x + DM + frow);
      const u16* wfp = (fmat ? wu : wg) + (size_t)frow * FF + 64 * fcb + 8 * (lane & 7);
      unsigned tl = 0;
      if (tw == 2 ? lane == 0 : ((lane & 7) == 0 && lane < 24))
        tl = __builtin_nontemporal_load((const GAS unsigned*)(tw == 2 ? Vc + kvbase : lane < 8 ? wfp : lane < 16 ? wop : wdp));
      WGBAR_A();
      asm volatile("" ::"v"(xa), "v"(xb), "v"(tl));
      lds_wait(cnt + 9, 4);
      TAGS_LDS();
      wait_until(t_0 + WGATE);
      v4u wf[8];
#pragma unroll
      for (int i = 0; i < 8; ++i) wf[i] = ldnt(wfp + (has_ft ? i * FF : 0));
      SB();
      float v[16];
#pragma unroll
      for (int e = 0; e < 16; ++e) v[e] = 0.f;
#pragma unroll
      for (int q = 0; q < 4; ++q) {
#pragma unroll
        for (int e = 0; e < 8; ++e) {
          const unsigned pk = permsel(wf[2 * q + 1][e >> 1], wf[2 * q][e >> 1], e);
          v[e] = dot2(pk, xa[q], v[e]);
          v[8 + e] = dot2(pk, xb[q], v[8 + e]);
        }
        SB();
      }
      const f32x4 fa = reduce16(v);
      if ((lane & 8) == 0) *(LAS f32x4*)(L + L_FRED + (((tw & 1) * 2 + (lane >> 5)) * 64 + 8 * (lane & 7) + 4 * ((lane >> 4) & 1)) * 4) = fa;
      const bool last = lds_signal_ret(cnt + 4) == 1u;
      const f32x2 a0 = *(LAS f32x2*)(L + L_FRED + (lane & 31) * 8) + *(LAS f32x2*)(L + L_FRED + 512 + (lane & 31) * 8);
      const f32x2 a1 = *(LAS f32x2*)(L + L_FRED + 256 + (lane & 31) * 8) + *(LAS f32x2*)(L + L_FRED + 768 + (lane & 31) * 8);
      asm volatile("buffer_store_dwordx4 %0, %1, %2, 0 offen sc1\n\ts_nop 1" ::"v"(pack4(f32x4{a0[0], a0[1], a1[0], a1[1]}, tag, tagB)),
                   "v"(last && has_ft && lane < 32 ? F1_OFF + ft * 512 + lane * 16 : OOB), "s"(rs) : "memory");
    }
    lds_wait(cnt + 10, 4);
#pragma unroll
    for (int i = 0; i < 4; ++i) wfr[i] = ldnt(wop + i * DM);
    SB();
    const float gate = __uint_as_float(cnt[13 + (lane >> 5)]);
    unsigned o0[4], o1[4], o2[4];
    const int rho = 2 * j + ((n >> 2) & 1);
    int oaw = L_OA + tw * 128 + (lane >> 5) * 64 + (lane & 7) * 8 + ((lane >> 4) & 1) * 4;
    int oar = L_OA + tw * 128 + (n >> 3) * 64 + rho * 8;
    int redw = (j & 1) == (n >> 3) ? L_RED + ((tw * 2 + (j & 1)) * 2 + (j >> 1)) * 256 + (n & 7) * 32 : L_DUMP + 4096 + lane * 32;
    v2u wb[8];
    {
      bool ho[4];
      v4u od[4];
      wait_until(t_0 + TGATE);
#pragma unroll
      for (int i = 0; i < 4; ++i) od[i] = ld_sc1(rs, s2off + i * 8 * 2048);
      SB();
#pragma unroll
      for (int e = 0; e < 8; ++e) {
        wb[e][0] = permsel(wfr[1][e >> 1], wfr[0][e >> 1], e);
        wb[e][1] = permsel(wfr[3][e >> 1], wfr[2][e >> 1], e);
        asm volatile("" : "+v"(wb[e]));
      }
      asm volatile("" : "+v"(oaw), "+v"(oar), "+v"(redw));
      SB();
#pragma unroll
      for (int i = 0; i < 4; ++i) {
        ho[i] = (od[i][1] ^ od[i][3]) == tgx;
        o0[i] = od[i][0]; o1[i] = od[i][1]; o2[i] = od[i][2];
      }
      unsigned spins = 0;
#pragma nounroll
      while (true) {
        const int miss = __builtin_popcountll(__ballot(!ho[0])) + __builtin_popcountll(__ballot(!ho[1])) +
                         __builtin_popcountll(__ballot(!ho[2])) + __builtin_popcountll(__ballot(!ho[3]));
        if (miss == 0) break;
        if (++spins >= SPIN_CAP) { alive = false; break; }
        if (miss > 128) __builtin_amdgcn_s_sleep(6);
        else if (miss > 40) __builtin_amdgcn_s_sleep(2);
        CBAR();
#pragma unroll
        for (int i = 0; i < 4; ++i) od[i] = ld_sc1(rs, ho[i] ? OOB : s2off + i * 8 * 2048);
        SB();
#pragma unroll
        for (int i = 0; i < 4; ++i) {
          o0[i] = ho[i] ? o0[i] : od[i][0];
          o1[i] = ho[i] ? o1[i] : od[i][1];
          o2[i] = ho[i] ? o2[i] : od[i][2];
          ho[i] = ho[i] | ((od[i][1] ^ od[i][3]) == tgx);
        }
      }
    }
    {
      float ls[4], ov[10];
#pragma unroll
      for (int i = 0; i < 4; ++i) ls[i] = __uint_as_float(o1[i] ^ tag);
      float mxb[2];
      {
        auto s0 = __builtin_amdgcn_permlane32_swap(__float_as_uint(fmaxf(ls[0], ls[1])), __float_as_uint(fmaxf(ls[2], ls[3])), false, false);
        float m = fmaxf(__uint_as_float(s0[0]), __uint_as_float(s0[1]));
        auto s1 = __builtin_amdgcn_permlane16_swap(__float_as_uint(m), __float_as_uint(m), false, false);
        m = fmaxf(__uint_as_float(s1[0]), __uint_as_float(s1[1]));
        m = fmaxf(m, dpp<D_ROR8>(m));
        auto s2 = __builtin_amdgcn_permlane32_swap(__float_as_uint(m), __float_as_uint(m), false, false);
        mxb[0] = __uint_as_float(s2[0]);
        mxb[1] = __uint_as_float(s2[1]);
      }
#pragma unroll
      for (int bb = 0; bb < 2; ++bb) {
        const float w0 = __builtin_amdgcn_exp2f(ls[2 * bb] - mxb[bb]), w1 = __builtin_amdgcn_exp2f(ls[2 * bb + 1] - mxb[bb]);
        ov[4 * bb + 0] = w0 * bflo(o0[2 * bb]) + w1 * bflo(o0[2 * bb + 1]);
        ov[4 * bb + 1] = w0 * bfhi(o0[2 * bb]) + w1 * bfhi(o0[2 * bb + 1]);
        ov[4 * bb + 2] = w0 * bflo(o2[2 * bb]) + w1 * bflo(o2[2 * bb + 1]);
        ov[4 * bb + 3] = w0 * bfhi(o2[2 * bb]) + w1 * bfhi(o2[2 * bb + 1]);
        ov[8 + bb] = w0 + w1;
      }
#pragma unroll
      for (int i = 0; i < 4; ++i) ov[i] = swapsum32(ov[i], ov[i + 4]);
      float W = swapsum32(ov[8], ov[9]);
      ov[0] = swapsum16(ov[0], ov[2]);
      ov[1] = swapsum16(ov[1], ov[3]);
      W = swapsum16(W, W);
      ov[0] += dpp<D_ROR8>(ov[0]);
      ov[1] += dpp<D_ROR8>(ov[1]);
      W += dpp<D_ROR8>(W);
      float sc = gate * __builtin_amdgcn_rcpf(W);
      if (!alive) sc = __uint_as_float(0x7FC00000u);
      *(LAS unsigned*)(L + oaw) = cvtpk(sc * ov[0], sc * ov[1]);
      CBAR();
      const v2u ao = *(LAS v2u*)(L + oar);
      float res[8];
#pragma unroll
      for (int e = 0; e < 8; ++e) res[e] = mfma16(ao, wb[e], zero4)[0];
      *(LAS f32x4*)(L + redw) = f32x4{res[0], res[1], res[2], res[3]};
      *(LAS f32x4*)(L + redw + 16) = f32x4{res[4], res[5], res[6], res[7]};
    }
    WGBAR();
    if (tw == 0) {
      f32x4 a = zero4;
#pragma unroll
      for (int t = 0; t < 5; ++t) a += *(LAS f32x4*)(L + L_RED + ((lane >> 5) * 5 + t) * 512 + (lane & 31) * 16);
      const f32x4 s = {swapsum32(a[0], a[0]), swapsum32(a[1], a[1]), swapsum32(a[2], a[2]), swapsum32(a[3], a[3])};
      if (lane < 32) st_sc1(rs, S3_OFF + (cblk * 32 + 4 * gc + hrel) * 512 + lane * 16, pack4(s, tag, tagB));
    } else if (tw == 1 && bid < 32) {
      v4u pd[4];
      unsigned spins = 0;
      bool done;
#pragma nounroll
      do {
        CBAR();
#pragma unroll
        for (int i = 0; i < 4; ++i) pd[i] = ld_sc1(rs, s3off + i * 4096);
        SB();
        done = __all(BAD4(pd) == 0u);
      } while (!done && ++spins < SPIN_CAP);
      alive = alive && done;
      const f32x4 a = USUM4(pd);
      float b0 = swapsum32(a[0], a[2]), b1 = swapsum32(a[1], a[3]);
      b0 = swapsum16(b0, b0);
      b1 = swapsum16(b1, b1);
      b0 += dpp<D_ROR8>(b0);
      b1 += dpp<D_ROR8>(b1);
      unsigned pk = cvtpk(b0, b1);
      if (!alive) pk = 0x7FC07FC0u;
      if ((lane & 24) == 0)
        __builtin_amdgcn_raw_buffer_store_b32(pk, __builtin_amdgcn_make_buffer_rsrc(out, 0, 2048, 0x00020000),
                                              (((bid >> 1) & 1) * DM + 64 * (bid >> 2) + 32 * (bid & 1) + 4 * (lane & 7) + 2 * (lane >> 5)) * 2, 0, 0);
      if (bid == 0 && lane == 0) __hip_atomic_store((GAS unsigned*)(ws + EPOCH_OFF), tag, RLX_AGENT);
    }
  }
}

}

extern "C" void kernel_launch(void* const* d_in, const int* in_sizes, int n_in, void* d_out, int out_size, void* d_ws,
                              size_t ws_size, hipStream_t stream) {
  using gm::u16;
  (void)in_sizes; (void)n_in; (void)out_size; (void)ws_size;
  gm::fused_kernel<<<gm::NWG, gm::NTHR, 0, stream>>>(
      (const u16*)d_in[0], (const u16*)d_in[3], (unsigned char*)d_ws, (const u16*)d_in[9], (const u16*)d_in[1],
      (const u16*)d_in[6], (const u16*)d_in[7], (const u16*)d_in[2], (const u16*)d_in[4], (const u16*)d_in[8], (u16*)d_out);
}
```

```cpp
#include <hip/hip_runtime.h>
#include <hip/hip_bf16.h>
#include <stdint.h>

namespace gm {

typedef unsigned v4u __attribute__((ext_vector_type(4)));
typedef unsigned v2u __attribute__((ext_vector_type(2)));
typedef float f32x4 __attribute__((ext_vector_type(4)));
typedef float f32x2 __attribute__((ext_vector_type(2)));
typedef __bf16 bf16x8_t __attribute__((ext_vector_type(8)));
typedef __bf16 bf16x2_t __attribute__((ext_vector_type(2)));
typedef unsigned short u16;

#define GAS __attribute__((address_space(1)))
#define LAS __attribute__((address_space(3)))
#define RLX_AGENT __ATOMIC_RELAXED, __HIP_MEMORY_SCOPE_AGENT

constexpr int SK = 32768, WINDOW = 4096, DH = 128, DM = 512, FF = 1792, HD = 4096;
constexpr int NWG = 256, NTHR = 768, NWAVES = 12;

constexpr int EPOCH_OFF = 0;
constexpr int QG_OFF = 4096;
constexpr int F1_OFF = QG_OFF + 256 * 512;
constexpr int S2_OFF = F1_OFF + 224 * 512;
constexpr int S3_OFF = S2_OFF + 8 * 32 * 2048;
constexpr int WS_END = S3_OFF + 8 * 32 * 512;

constexpr int L_CNT = 0;
constexpr int L_QRED = 64;
constexpr int L_FRED = L_QRED + 2048;
constexpr int L_Q = L_FRED + 1024;
constexpr int L_H = L_Q + 1024;
constexpr int L_OA = L_H + 256;
constexpr int L_WMAX = L_OA + 512;
constexpr int L_LSUM = L_WMAX + 64;
constexpr int L_RED = L_LSUM + 64;
constexpr int L_P = L_RED + 5120;
constexpr int L_OP = L_P + 8192;
constexpr int OP_W = 2304;
constexpr int L_K = L_OP + 4 * OP_W;
constexpr int L_DUMP = L_K;
constexpr int L_END = L_K + 4 * 16384;
constexpr int LDS_BYTES = 100 * 1024;
static_assert(L_END <= LDS_BYTES && (L_OP % 16) == 0 && (L_K % 16) == 0 && (L_RED % 16) == 0 && (L_P % 16) == 0, "lds map");

constexpr unsigned SPIN_CAP = 1u << 18;
constexpr int AUX_SC1 = 16;
constexpr int OOB = 0x7FFFFF00;

__device__ __forceinline__ v4u ldnt(const void* p) { return __builtin_nontemporal_load((const GAS v4u*)p); }
__device__ __forceinline__ float dot2(unsigned a, unsigned b, float c) {
  return __builtin_amdgcn_fdot2_f32_bf16(__builtin_bit_cast(bf16x2_t, a), __builtin_bit_cast(bf16x2_t, b), c, false);
}
__device__ __forceinline__ unsigned cvtpk(float lo, float hi) {
  bf16x2_t r = {(__bf16)lo, (__bf16)hi};
  return __builtin_bit_cast(unsigned, r);
}
__device__ __forceinline__ float bfround(float v) { return (float)(__bf16)v; }
__device__ __forceinline__ float bflo(unsigned u) { return __uint_as_float(u << 16); }
__device__ __forceinline__ float bfhi(unsigned u) { return __uint_as_float(u & 0xffff0000u); }
template <int CTRL> __device__ __forceinline__ float dpp(float x) {
  return __builtin_bit_cast(float, __builtin_amdgcn_mov_dpp(__builtin_bit_cast(int, x), CTRL, 0xf, 0xf, true));
}
constexpr int D_XOR1 = 0xB1, D_XOR2 = 0x4E, D_ROR8 = 0x128, D_ROR4 = 0x124;
__device__ __forceinline__ float swapsum32(float a, float b) {
  auto s = __builtin_amdgcn_permlane32_swap(__float_as_uint(a), __float_as_uint(b), false, false);
  return __uint_as_float(s[0]) + __uint_as_float(s[1]);
}
__device__ __forceinline__ float swapsum16(float a, float b) {
  auto s = __builtin_amdgcn_permlane16_swap(__float_as_uint(a), __float_as_uint(b), false, false);
  return __uint_as_float(s[0]) + __uint_as_float(s[1]);
}
__device__ __forceinline__ float xrow_max(float x) {
  auto s = __builtin_amdgcn_permlane16_swap(__float_as_uint(x), __float_as_uint(x), false, false);
  x = fmaxf(__uint_as_float(s[0]), __uint_as_float(s[1]));
  auto t = __builtin_amdgcn_permlane32_swap(__float_as_uint(x), __float_as_uint(x), false, false);
  return fmaxf(__uint_as_float(t[0]), __uint_as_float(t[1]));
}
__device__ __forceinline__ float max4(f32x4 a) { return fmaxf(fmaxf(a[0], a[1]), fmaxf(a[2], a[3])); }
__device__ __forceinline__ float sum4(f32x4 a) { return (a[0] + a[1]) + (a[2] + a[3]); }
__device__ __forceinline__ f32x4 mfma32(v4u a, v4u b, f32x4 c) {
  return __builtin_amdgcn_mfma_f32_16x16x32_bf16(__builtin_bit_cast(bf16x8_t, a), __builtin_bit_cast(bf16x8_t, b), c, 0, 0, 0);
}
typedef short s16x4 __attribute__((ext_vector_type(4)));
__device__ __forceinline__ f32x4 mfma16(v2u a, v2u b, f32x4 c) {
  return __builtin_amdgcn_mfma_f32_16x16x16bf16_1k(__builtin_bit_cast(s16x4, a), __builtin_bit_cast(s16x4, b), c, 0, 0, 0);
}
__device__ __forceinline__ unsigned permsel(unsigned hi, unsigned lo, int e) {
  return __builtin_amdgcn_perm(hi, lo, (e & 1) ? 0x07060302u : 0x05040100u);
}
__device__ __forceinline__ v4u ld_sc1(__amdgpu_buffer_rsrc_t rs, int off) {
  return __builtin_amdgcn_raw_buffer_load_b128(rs, off, 0, AUX_SC1);
}
__device__ __forceinline__ void st_sc1(__amdgpu_buffer_rsrc_t rs, int off, v4u v) {
  __builtin_amdgcn_raw_buffer_store_b128(v, rs, off, 0, AUX_SC1);
}
__device__ __forceinline__ unsigned tag_b(unsigned t) { return (t << 1) ^ ((t >> 31) ? 0x04C11DB7u : 0u); }
__device__ __forceinline__ v4u pack4(f32x4 v, unsigned tA, unsigned tB) {
  const unsigned u3 = __float_as_uint(v[3]);
  const unsigned a = __builtin_amdgcn_perm(__float_as_uint(v[0]), u3, 0x07060503u);
  const unsigned b = __builtin_amdgcn_perm(__float_as_uint(v[1]), u3, 0x07060502u);
  const unsigned c = __builtin_amdgcn_perm(__float_as_uint(v[2]), u3, 0x07060501u);
  return v4u{a, c ^ tA, b, c ^ tB};
}
__device__ __forceinline__ f32x4 unpack4(v4u p, unsigned tA) {
  const unsigned c = p[1] ^ tA;
  const unsigned t = __builtin_amdgcn_perm(p[0], p[2], 0x04000c0cu);
  const unsigned u3 = __builtin_amdgcn_perm(t, c, 0x0706000cu);
  return f32x4{__uint_as_float(p[0]), __uint_as_float(p[2]), __uint_as_float(c), __uint_as_float(u3)};
}
#define SB() __builtin_amdgcn_sched_barrier(0)
#define WGBAR()                                                     \
  do {                                                              \
    SB();                                                           \
    asm volatile("s_waitcnt lgkmcnt(0)\n\ts_barrier" ::: "memory"); \
    SB();                                                           \
  } while (0)
#define CBAR() asm volatile("" ::: "memory")
__device__ __forceinline__ void lds_signal(LAS unsigned* p) {
  if ((threadIdx.x & 63) == 0) asm volatile("ds_add_u32 %0, %1" ::"v"((unsigned)(uintptr_t)p), "v"(1u) : "memory");
  CBAR();
}
__device__ __forceinline__ unsigned lds_signal_ret(LAS unsigned* p) {
  unsigned old = 0;
  if ((threadIdx.x & 63) == 0)
    asm volatile("ds_add_rtn_u32 %0, %1, %2\n\ts_waitcnt lgkmcnt(0)" : "=&v"(old) : "v"((unsigned)(uintptr_t)p), "v"(1u) : "memory");
  old = __builtin_amdgcn_readfirstlane(old);
  CBAR();
  return old;
}
__device__ __forceinline__ void lds_wait(LAS unsigned* p, unsigned want) {
  SB();
  while (__builtin_amdgcn_readfirstlane(*(volatile LAS unsigned*)p) < want) __builtin_amdgcn_s_sleep(1);
  CBAR();
  SB();
}
__device__ __forceinline__ void lds_wait_fast(LAS unsigned* p, unsigned want) {
  SB();
  while (__builtin_amdgcn_readfirstlane(*(volatile LAS unsigned*)p) < want) {}
  CBAR();
  SB();
}
__device__ __forceinline__ void wait_until(unsigned long long t) {
  while (__builtin_amdgcn_s_memrealtime() < t) __builtin_amdgcn_s_sleep(1);
}
__device__ __forceinline__ f32x4 reduce16(float (&v)[16]) {
#pragma unroll
  for (int i = 0; i < 8; ++i) v[i] = swapsum32(v[i], v[i + 8]);
#pragma unroll
  for (int i = 0; i < 4; ++i) v[i] = swapsum16(v[i], v[i + 4]);
#pragma unroll
  for (int i = 0; i < 4; ++i) v[i] += dpp<D_ROR8>(v[i]);
  return f32x4{v[0], v[1], v[2], v[3]};
}


constexpr int PS0 = 42, PS1 = 16;
constexpr int K0 = 8;
constexpr int VGATE = 358, VSTEP = 13;
constexpr int WGATE = 520;
constexpr int TGATE = 665;
constexpr int HGATE = 700;
constexpr int CODE_LINES = 123;
#define BAD4(a) ((((a)[0][1] ^ (a)[0][3]) ^ tgx) | (((a)[1][1] ^ (a)[1][3]) ^ tgx) | (((a)[2][1] ^ (a)[2][3]) ^ tgx) | (((a)[3][1] ^ (a)[3][3]) ^ tgx))
#define USUM4(a) ((unpack4((a)[0], tag) + unpack4((a)[1], tag)) + (unpack4((a)[2], tag) + unpack4((a)[3], tag)))

__global__ void __launch_bounds__(NTHR, 3)
fused_kernel(const u16* x, const u16* wq_, unsigned char* ws, const u16* gw, const u16* Kc, const u16* wg, const u16* wu,
             const u16* Vc, const u16* wo, const u16* wd, u16* out) {
  __shared__ __attribute__((aligned(16))) unsigned char lds_raw[LDS_BYTES];
  unsigned long long p_x, p_wq, p_ws, pc0;
  asm volatile("s_load_dwordx2 %0, %4, 0x0\n\ts_load_dwordx2 %1, %4, 0x8\n\ts_load_dwordx2 %2, %4, 0x10\n\ts_getpc_b64 %3"
               : "=&s"(p_x), "=&s"(p_wq), "=&s"(p_ws), "=&s"(pc0)
               : "s"(__builtin_amdgcn_kernarg_segment_ptr()));
  SB();
  LAS unsigned char* L = (LAS unsigned char*)lds_raw;
  LAS unsigned* cnt = (LAS unsigned*)(L + L_CNT);
  const int tid = threadIdx.x, lane = tid & 63;
  const int w = __builtin_amdgcn_readfirstlane(tid >> 6);
  const int bid = blockIdx.x, grp = bid & 7, r = bid >> 3;
  unsigned codew, epoch;
  v2u xq0, xq1;
  v4u big[4];
  {
    const int qrow = 128 * (r >> 3) + 32 * w + 4 * (lane >> 3);
    const unsigned qo = (qrow * HD + 512 * grp + 64 * (r & 7) + 8 * (lane & 7)) * 2;
    const int cl = 4 * r + (lane & 3);
    const unsigned coff = (cl < CODE_LINES ? cl : 0) * 128;
    asm volatile(
        "s_cmp_lg_u32 %[w], 9\n\ts_cbranch_scc1 1f\n\t"
        "global_load_dword %[cw], %[coff], %[pc]\n\t"
        "s_waitcnt lgkmcnt(0)\n\t"
        "global_load_dword %[ep], %[vz], %[pws] sc1\n"
        "1:\n\ts_cmp_gt_u32 %[w], 3\n\ts_cbranch_scc1 2f\n\t"
        "s_waitcnt lgkmcnt(0)\n\t"
        "global_load_dwordx4 %[b0], %[o0], %[pb] nt\n\t"
        "global_load_dwordx4 %[b1], %[o1], %[pb] nt\n\t"
        "global_load_dwordx4 %[b2], %[o2], %[pb] nt\n\t"
        "global_load_dwordx4 %[b3], %[o3], %[pb] nt\n\t"
        "global_load_dwordx2 %[xa], %[xoff], %[px]\n\t"
        "global_load_dwordx2 %[xb], %[xoff], %[px] offset:1024\n"
        "2:"
        : [cw] "=&v"(codew), [ep] "=&v"(epoch), [xa] "=&v"(xq0), [xb] "=&v"(xq1), [b0] "=&v"(big[0]), [b1] "=&v"(big[1]),
          [b2] "=&v"(big[2]), [b3] "=&v"(big[3])
        : [w] "s"(w), [coff] "v"(coff), [pc] "s"(pc0 & ~127ull), [xoff] "v"(qrow * 2), [px] "s"(p_x), [vz] "v"(0), [pws] "s"(p_ws),
          [pb] "s"(p_wq), [o0] "v"(qo), [o1] "v"(qo + HD * 2), [o2] "v"(qo + HD * 4), [o3] "v"(qo + HD * 6)
        : "memory", "scc");
  }
#define WGBAR_A()                                                   \
  do {                                                              \
    WGBAR();                                                        \
    asm volatile("" ::"s"(p_x), "s"(p_wq), "s"(p_ws));   \
  } while (0)
#define PRO_WAIT(N)                                             \
  do {                                                          \
    SB();                                                       \
    asm volatile("s_waitcnt vmcnt(" #N ")" ::: "memory");       \
    SB();                                                       \
  } while (0)
  const int sw = grp * 2 + (r >> 4);
  const size_t kvbase = ((size_t)((r >> 4) * 8 + grp) * SK + (SK - WINDOW)) * DH + (4 * (r & 15) + (w & 3)) * 512;
  const int gc = grp ^ 1;
  const int j = lane >> 4, n = lane & 15;
#define MAKE_RS() asm volatile("" : "+s"(ws)); const __amdgpu_buffer_rsrc_t rs = __builtin_amdgcn_make_buffer_rsrc(ws, 0, WS_END, 0x00020000)
  const f32x4 zero4 = {0.f, 0.f, 0.f, 0.f};
  constexpr float LOG2E = 1.4426950408889634f;

  if (w < 4) {
    const unsigned long long t_v = __builtin_amdgcn_s_memrealtime();
    WGBAR_A();
    PRO_WAIT(0);
    asm volatile("" : "+v"(xq0), "+v"(xq1), "+v"(big[0]), "+v"(big[1]), "+v"(big[2]), "+v"(big[3]));
    {
      float v[16];
#pragma unroll
      for (int e = 0; e < 8; ++e) {
        const unsigned p01 = permsel(big[1][e >> 1], big[0][e >> 1], e), p23 = permsel(big[3][e >> 1], big[2][e >> 1], e);
        v[e] = dot2(p23, xq0[1], dot2(p01, xq0[0], 0.f));
        v[8 + e] = dot2(p23, xq1[1], dot2(p01, xq1[0], 0.f));
      }
      const f32x4 a = reduce16(v);
      if ((lane & 8) == 0) *(LAS f32x4*)(L + L_QRED + ((w * 2 + (lane >> 5)) * 64 + 8 * (lane & 7) + 4 * ((lane >> 4) & 1)) * 4) = a;
    }
    lds_signal(cnt + 6);
    MAKE_RS();
    v4u vf[16];
    lds_wait(cnt + 7, 2);
#pragma unroll
    for (int i = 0; i < 16; ++i) {
      if (i >= 4 && (i & 1) == 0) { SB(); wait_until(t_v + VGATE + (i - 4) * VSTEP); SB(); }
      vf[i] = ldnt(Vc + kvbase + (i ^ sw) * 32768 + lane * 8);
    }
    SB();
    lds_signal(cnt + 9);
    v4u be[8];
#pragma unroll
    for (int q = 0; q < 4; ++q) {
#pragma unroll
      for (int e = 0; e < 8; ++e) be[e][q] = permsel(vf[2 * q + 1][e >> 1], vf[2 * q][e >> 1], e);
      SB();
    }
#pragma unroll
    for (int e = 0; e < 8; ++e) asm volatile("" : "+v"(be[e]));
    lds_wait(cnt + 5, 4);
    __builtin_amdgcn_s_setprio(3);
    const v4u pa0 = *(LAS v4u*)(L + L_P + (w * 2) * 1024 + lane * 16), pa1 = *(LAS v4u*)(L + L_P + (w * 2 + 1) * 1024 + lane * 16);
    const float Ls = sum4(*(LAS f32x4*)(L + L_LSUM + w * 16));
    const float Mh = max4(*(LAS f32x4*)(L + L_WMAX + w * 16));
    const unsigned tag = cnt[12];
    f32x4 oacc[8];
#pragma unroll
    for (int e = 0; e < 8; ++e) oacc[e] = mfma32(pa0, be[e], zero4);
    float inv = __builtin_amdgcn_rcpf(Ls);
    const unsigned lse = __float_as_uint(fmaf(Mh, LOG2E, __builtin_amdgcn_logf(Ls)));
    unsigned lA = lse ^ tag, lB = lse ^ tag_b(tag);
    int s2dst = lane < 32 ? S2_OFF + (grp * 32 + r) * 2048 + w * 512 + lane * 16 : OOB;
    int src = L_OP + (lane >> 5) * 2 * OP_W + (lane & 1) * 1152 + (w * 16 + ((lane & 31) >> 1)) * 16;
    asm volatile("" : "+v"(inv), "+v"(lA), "+v"(lB), "+v"(s2dst), "+v"(src));
    SB();
#pragma unroll
    for (int q = 0; q < 4; ++q) {
#pragma unroll
      for (int e = 0; e < 8; ++e) be[e][q] = permsel(vf[8 + 2 * q + 1][e >> 1], vf[8 + 2 * q][e >> 1], e);
      if (q < 3) SB();
    }
#pragma unroll
    for (int e = 0; e < 8; ++e) oacc[e] = mfma32(pa1, be[e], oacc[e]);
    *(LAS f32x4*)(L + L_OP + w * OP_W + lane * 16) = f32x4{oacc[0][0], oacc[1][0], oacc[2][0], oacc[3][0]};
    *(LAS f32x4*)(L + L_OP + w * OP_W + 1152 + lane * 16) = f32x4{oacc[4][0], oacc[5][0], oacc[6][0], oacc[7][0]};
    if (lds_signal_ret(cnt + 0) != 3u) lds_wait_fast(cnt + 0, 4);
    {
      const f32x4 t = *(LAS f32x4*)(L + src) + *(LAS f32x4*)(L + src + OP_W);
      const float a0 = swapsum32(t[0], t[0]), a1 = swapsum32(t[1], t[1]), a2 = swapsum32(t[2], t[2]), a3 = swapsum32(t[3], t[3]);
      st_sc1(rs, s2dst, v4u{cvtpk(a0 * inv, a1 * inv), lA, cvtpk(a2 * inv, a3 * inv), lB});
    }
    lds_signal(cnt + 10);
    return;
  }

  if (w < 8) {
    __builtin_amdgcn_s_setprio(2);
    const int k = w - 4;
    const unsigned long long t_gate = __builtin_amdgcn_s_memrealtime() + HGATE;
    const u16* kp = Kc + kvbase + lane * 8;
    WGBAR_A();
    v4u kf[16];
#pragma unroll
    for (int i = 0; i < K0; ++i) kf[i] = ldnt(kp + (i ^ sw) * 32768);
    SB();
    lds_wait(cnt + 8, 1);
#pragma unroll
    for (int i = K0; i < 16; ++i) kf[i] = ldnt(kp + (i ^ sw) * 32768);
    SB();
    LAS unsigned char* Kl = L + L_K + k * 16384;
#pragma unroll
    for (int i = 0; i < 16; ++i) {
      const int key = 4 * i + j;
      *(LAS v4u*)(Kl + key * 256 + ((n ^ (key & 15)) << 4)) = kf[i];
    }
    v4u ka[4][4];
    {
      const int kin = 4 * (n & 3) + (n >> 2);
#pragma unroll
      for (int t = 0; t < 4; ++t)
#pragma unroll
        for (int st = 0; st < 4; ++st) ka[t][st] = *(LAS v4u*)(Kl + (16 * t + kin) * 256 + (((4 * st + j) ^ kin) << 4));
    }
    lds_wait(cnt + 2, 2);
    v4u qf[4];
#pragma unroll
    for (int st = 0; st < 4; ++st) qf[st] = *(LAS v4u*)(L + L_Q + (n >> 2) * 256 + (4 * st + j) * 16);
    f32x4 sc[4];
#pragma unroll
    for (int t = 0; t < 4; ++t) sc[t] = zero4;
#pragma unroll
    for (int st = 0; st < 4; ++st)
#pragma unroll
      for (int t = 0; t < 4; ++t) sc[t] = mfma32(ka[t][st], qf[st], sc[t]);
#pragma unroll
    for (int t = 0; t < 4; ++t) {
      const unsigned a = cvtpk(sc[t][0], sc[t][1]), b = cvtpk(sc[t][2], sc[t][3]);
      const unsigned c = cvtpk(bflo(a) * 0.08838834764831845f, bfhi(a) * 0.08838834764831845f);
      const unsigned d = cvtpk(bflo(b) * 0.08838834764831845f, bfhi(b) * 0.08838834764831845f);
      sc[t] = f32x4{bflo(c), bfhi(c), bflo(d), bfhi(d)};
    }
    const float mx = xrow_max(fmaxf(fmaxf(max4(sc[0]), max4(sc[1])), fmaxf(max4(sc[2]), max4(sc[3]))));
    if (lane < 16 && (lane & 3) == 0) *(LAS float*)(L + L_WMAX + (lane + k) * 4) = mx;
    if (lds_signal_ret(cnt + 1) != 3u) lds_wait_fast(cnt + 1, 4);
    const float Mc = LOG2E * max4(*(LAS f32x4*)(L + L_WMAX + (n >> 2) * 16));
    v4u pa[2];
#pragma unroll
    for (int t = 0; t < 4; ++t) {
      float p[4];
#pragma unroll
      for (int i = 0; i < 4; ++i) p[i] = __builtin_amdgcn_exp2f(fmaf(sc[t][i], LOG2E, -Mc));
      pa[t >> 1][2 * (t & 1)] = cvtpk(p[0], p[1]);
      pa[t >> 1][2 * (t & 1) + 1] = cvtpk(p[2], p[3]);
    }
    *(LAS v4u*)(L + L_P + (k * 2) * 1024 + lane * 16) = pa[0];
    *(LAS v4u*)(L + L_P + (k * 2 + 1) * 1024 + lane * 16) = pa[1];
    const v4u ones = {0x3F803F80u, 0x3F803F80u, 0x3F803F80u, 0x3F803F80u};
    const f32x4 ls = mfma32(pa[1], ones, mfma32(pa[0], ones, zero4));
    if (n == 0) *(LAS float*)(L + L_LSUM + (j * 4 + k) * 4) = ls[0];
    lds_signal(cnt + 5);
    if (k == 0) {
      MAKE_RS();
      const unsigned tag = cnt[12], tgx = tag ^ tag_b(tag);
      const int cc = 224 * gc + 56 * (r >> 3) + 2 * ((lane & 31) < 28 ? (lane & 31) : 27);
      const int hoff = F1_OFF + ((lane >> 5) * 28 + (cc >> 6)) * 2048 + (cc & 63) * 8;
      const float omg = bfround(1.0f - __uint_as_float(cnt[13 + (lane >> 5)]));
      const u16* wdp = wd + (size_t)(224 * gc + 56 * (r >> 3) + 7 * (lane >> 3)) * DM + 64 * (r & 7) + 8 * (lane & 7);
      const int c0 = 2 * (lane & 31), c1 = c0 + 1;
      int hw0 = (lane & 31) < 28 ? L_H + (lane >> 5) * 128 + (c0 / 7) * 16 + (c0 % 7) * 2 : L_DUMP + lane * 4;
      int hw1 = (lane & 31) < 28 ? L_H + (lane >> 5) * 128 + (c1 / 7) * 16 + (c1 % 7) * 2 : L_DUMP + lane * 4 + 2;
      int har = L_H + (n >> 3) * 128 + (2 * j + ((n >> 2) & 1)) * 16;
      int redw = (j & 1) == (n >> 3) ? L_RED + ((8 + (j & 1)) * 2 + (j >> 1)) * 256 + (n & 7) * 32 : L_DUMP + 1024 + lane * 32;
      if (lane < 16) *(LAS u16*)(L + L_H + lane * 16 + 14) = 0;
      lds_wait(cnt + 10, 4);
      v4u dfr[7], db[8];
#pragma unroll
      for (int i = 0; i < 7; ++i) dfr[i] = ldnt(wdp + i * DM);
      SB();
      wait_until(t_gate);
      v4u hg[4];
      bool hh[4];
#pragma unroll
      for (int p = 0; p < 4; ++p) hg[p] = ld_sc1(rs, hoff + p * 512);
      SB();
#pragma unroll
      for (int e = 0; e < 8; ++e) {
        db[e][0] = permsel(dfr[1][e >> 1], dfr[0][e >> 1], e);
        db[e][1] = permsel(dfr[3][e >> 1], dfr[2][e >> 1], e);
        db[e][2] = permsel(dfr[5][e >> 1], dfr[4][e >> 1], e);
        db[e][3] = permsel(0u, dfr[6][e >> 1], e);
        asm volatile("" : "+v"(db[e]));
      }
      asm volatile("" : "+v"(hw0), "+v"(hw1), "+v"(har), "+v"(redw));
      SB();
#pragma unroll
      for (int p = 0; p < 4; ++p) hh[p] = (hg[p][1] ^ hg[p][3]) == tgx;
      bool done = __all(hh[0] & hh[1] & hh[2] & hh[3]);
#pragma nounroll
      for (unsigned spins = 0; !done && spins < SPIN_CAP; ++spins) {
        __builtin_amdgcn_s_sleep(2);
        CBAR();
        v4u t[4];
#pragma unroll
        for (int p = 0; p < 4; ++p) t[p] = ld_sc1(rs, hh[p] ? OOB : hoff + p * 512);
        SB();
#pragma unroll
        for (int p = 0; p < 4; ++p) {
          hg[p][0] = hh[p] ? hg[p][0] : t[p][0];
          hg[p][1] = hh[p] ? hg[p][1] : t[p][1];
          hg[p][2] = hh[p] ? hg[p][2] : t[p][2];
          hh[p] = hh[p] | ((t[p][1] ^ t[p][3]) == tgx);
        }
        done = __all(hh[0] & hh[1] & hh[2] & hh[3]);
      }
      const f32x4 hsum = USUM4(hg);
      float hv2[2];
#pragma unroll
      for (int c = 0; c < 2; ++c) {
        auto sres = __builtin_amdgcn_permlane32_swap(__float_as_uint(hsum[c]), __float_as_uint(hsum[2 + c]), false, false);
        const float gb = bfround(__uint_as_float(sres[0]));
        const float si = bfround(gb * __builtin_amdgcn_rcpf(1.0f + __expf(-gb)));
        hv2[c] = bfround(si * bfround(__uint_as_float(sres[1]))) * omg;
      }
      unsigned hp = cvtpk(hv2[0], hv2[1]);
      if (!done) hp = 0x7FC07FC0u;
      *(LAS u16*)(L + hw0) = (u16)hp;
      *(LAS u16*)(L + hw1) = (u16)(hp >> 16);
      CBAR();
      const v4u ha = *(LAS v4u*)(L + har);
      float res[8];
#pragma unroll
      for (int e = 0; e < 8; ++e) res[e] = mfma32(ha, db[e], zero4)[0];
      *(LAS f32x4*)(L + redw) = f32x4{res[0], res[1], res[2], res[3]};
      *(LAS f32x4*)(L + redw + 16) = f32x4{res[4], res[5], res[6], res[7]};
      WGBAR();
      return;
    }
    return;
  }

  {
    __builtin_amdgcn_s_setprio(3);
    const int tw = w - 8;
    MAKE_RS();
    const unsigned long long t_0 = __builtin_amdgcn_s_memrealtime();
    const int cblk = r & 7, hrel = r >> 3;
    const u16* wop = wo + (size_t)(512 * gc + 128 * hrel + 32 * tw + 4 * (lane >> 3)) * DM + 64 * cblk + 8 * (lane & 7);
    const u16* wdp = wd + (size_t)(224 * gc + 56 * hrel) * DM + 64 * cblk;
    int s2off = S2_OFF + (gc * 32 + (lane >> 3)) * 2048 + hrel * 512 + tw * 128 + (lane & 7) * 16;
    int s3off = S3_OFF + ((bid >> 2) * 32 + (lane >> 3)) * 512 + (bid & 3) * 128 + (lane & 7) * 16;
    asm volatile("" : "+v"(s2off), "+v"(s3off));
    v4u wfr[4];
#define TAGS_LDS()                                                                \
  do {                                                                            \
    tag = cnt[12];                                                                \
    tagB = tag_b(tag);                                                            \
    tgx = tag ^ tagB;                                                             \
  } while (0)
#define TAGS()                                                                    \
  do {                                                                            \
    asm volatile("" : "+v"(epoch));                                               \
    unsigned tg = epoch * 1664525u + 1013904223u;   \
    if (tg == 0u) tg = 1013904223u;                 \
    tag = tg;                                                                     \
    tagB = tag_b(tg);                                                             \
    tgx = tag ^ tagB;                                                             \
    asm volatile("" : "+v"(tag), "+v"(tagB), "+v"(tgx));                          \
  } while (0)
    unsigned tag, tagB, tgx;
    bool alive = true;
    const bool has_ft = r < 28;
    const int ft = grp * 28 + (r < 28 ? r : 0);
    const int fmat = ft >= 112, fcb = (ft - 112 * fmat) >> 2, frb = ft & 3;
    if (tw < 2) {
      if (tw == 1) {
        if (lane < 16) cnt[lane] = 0u;
        WGBAR_A();
        PRO_WAIT(0);
        asm volatile("" ::"v"(codew));
        TAGS();
        if (lane == 0) cnt[12] = tag;
        lds_wait(cnt + 6, 4);
        f32x4 a = *(LAS f32x4*)(L + L_QRED + (lane >> 5) * 1024 + (lane & 31) * 16) + *(LAS f32x4*)(L + L_QRED + (lane >> 5) * 1024 + 512 + (lane & 31) * 16);
        a = f32x4{swapsum32(a[0], a[0]), swapsum32(a[1], a[1]), swapsum32(a[2], a[2]), swapsum32(a[3], a[3])};
        st_sc1(rs, lane < 32 ? QG_OFF + ((grp * 8 + (r & 7)) * 4 + (r >> 3)) * 512 + lane * 16 : OOB, pack4(a, tag, tagB));
        lds_signal(cnt + 8);
      } else {
        const v4u gv = *(const GAS v4u*)(gw + 8 * lane);
        const v4u xa = *(const GAS v4u*)(x + 8 * lane), xb = *(const GAS v4u*)(x + DM + 8 * lane);
        WGBAR_A();
        float a0 = 0.f, a1 = 0.f;
#pragma unroll
        for (int q = 0; q < 4; ++q) {
          a0 += bfround(bflo(gv[q]) * bflo(xa[q])) + bfround(bfhi(gv[q]) * bfhi(xa[q]));
          a1 += bfround(bflo(gv[q]) * bflo(xb[q])) + bfround(bfhi(gv[q]) * bfhi(xb[q]));
        }
        float a = swapsum32(a0, a1);
        a = swapsum16(a, a);
        a += dpp<D_ROR8>(a);
        a += dpp<D_ROR4>(a);
        a += dpp<D_XOR2>(a);
        a += dpp<D_XOR1>(a);
        const float gt = bfround(__builtin_amdgcn_rcpf(1.0f + __expf(-bfround(a))));
        if ((lane & 31) == 0) cnt[13 + (lane >> 5)] = __float_as_uint(gt);
        lds_wait(cnt + 8, 1);
        TAGS_LDS();
      }
      const int qoff = QG_OFF + (grp * 8 + 4 * tw + (lane >> 4)) * 2048 + (r >> 4) * 256 + (lane & 15) * 16;
      v4u pq[2][4];
#pragma unroll
      for (int kk = 0; kk < 2; ++kk) {
        if (kk == 0) __builtin_amdgcn_s_sleep(PS0);
        else __builtin_amdgcn_s_sleep(PS1);
        CBAR();
#pragma unroll
        for (int p = 0; p < 4; ++p) pq[kk][p] = ld_sc1(rs, qoff + p * 512);
        SB();
        if (kk == 0) lds_signal(cnt + 7);
      }
      f32x4 qv;
      if (__all(BAD4(pq[0]) == 0u)) {
        qv = USUM4(pq[0]);
      } else {
        bool done = __all(BAD4(pq[1]) == 0u);
        unsigned spins = 0;
#pragma nounroll
        while (!done && ++spins < SPIN_CAP) {
          CBAR();
#pragma unroll
          for (int p = 0; p < 4; ++p) pq[1][p] = ld_sc1(rs, qoff + p * 512);
          done = __all(BAD4(pq[1]) == 0u);
        }
        alive = done;
        qv = USUM4(pq[1]);
      }
      *(LAS v2u*)(L + L_Q + tw * 512 + lane * 8) = v2u{cvtpk(qv[0], qv[1]), cvtpk(qv[2], qv[3])};
      lds_signal(cnt + 2);
      asm volatile("" ::"v"(pq[1][0]), "v"(pq[1][1]), "v"(pq[1][2]), "v"(pq[1][3]));
      lds_wait(cnt + 9, 4);
      pq[0][1] = ld_sc1(rs, bid < 32 ? s3off + tw * 8192 : OOB);
      pq[0][2] = ld_sc1(rs, bid < 32 ? s3off + tw * 8192 + 4096 : OOB);
      asm volatile("" ::"v"(pq[0][1]), "v"(pq[0][2]));
    } else {
      const int frow = 128 * frb + 64 * (tw & 1) + 8 * (lane >> 3);
      const v4u xa = *(const GAS v4u*)(x + frow), xb = *(const GAS v4u*)(x + DM + frow);
      const u16* wfp = (fmat ? wu : wg) + (size_t)frow * FF + 64 * fcb + 8 * (lane & 7);
      unsigned tl = 0;
      if (tw == 2 ? lane == 0 : ((lane & 7) == 0 && lane < 24))
        tl = __builtin_nontemporal_load((const GAS unsigned*)(tw == 2 ? Vc + kvbase : lane < 8 ? wfp : lane < 16 ? wop : wdp));
      WGBAR_A();
      asm volatile("" ::"v"(xa), "v"(xb), "v"(tl));
      lds_wait(cnt + 9, 4);
      TAGS_LDS();
      wait_until(t_0 + WGATE);
      v4u wf[8];
#pragma unroll
      for (int i = 0; i < 8; ++i) wf[i] = ldnt(wfp + (has_ft ? i * FF : 0));
      SB();
      float v[16];
#pragma unroll
      for (int e = 0; e < 16; ++e) v[e] = 0.f;
#pragma unroll
      for (int q = 0; q < 4; ++q) {
#pragma unroll
        for (int e = 0; e < 8; ++e) {
          const unsigned pk = permsel(wf[2 * q + 1][e >> 1], wf[2 * q][e >> 1], e);
          v[e] = dot2(pk, xa[q], v[e]);
          v[8 + e] = dot2(pk, xb[q], v[8 + e]);
        }
        SB();
      }
      const f32x4 fa = reduce16(v);
      if ((lane & 8) == 0) *(LAS f32x4*)(L + L_FRED + (((tw & 1) * 2 + (lane >> 5)) * 64 + 8 * (lane & 7) + 4 * ((lane >> 4) & 1)) * 4) = fa;
      const bool last = lds_signal_ret(cnt + 4) == 1u;
      const f32x2 a0 = *(LAS f32x2*)(L + L_FRED + (lane & 31) * 8) + *(LAS f32x2*)(L + L_FRED + 512 + (lane & 31) * 8);
      const f32x2 a1 = *(LAS f32x2*)(L + L_FRED + 256 + (lane & 31) * 8) + *(LAS f32x2*)(L + L_FRED + 768 + (lane & 31) * 8);
      asm volatile("buffer_store_dwordx4 %0, %1, %2, 0 offen sc1\n\ts_nop 1" ::"v"(pack4(f32x4{a0[0], a0[1], a1[0], a1[1]}, tag, tagB)),
                   "v"(last && has_ft && lane < 32 ? F1_OFF + ft * 512 + lane * 16 : OOB), "s"(rs) : "memory");
    }
    lds_wait(cnt + 10, 4);
#pragma unroll
    for (int i = 0; i < 4; ++i) wfr[i] = ldnt(wop + i * DM);
    SB();
    const float gate = __uint_as_float(cnt[13 + (lane >> 5)]);
    unsigned o0[4], o1[4], o2[4];
    const int rho = 2 * j + ((n >> 2) & 1);
    int oaw = L_OA + tw * 128 + (lane >> 5) * 64 + (lane & 7) * 8 + ((lane >> 4) & 1) * 4;
    int oar = L_OA + tw * 128 + (n >> 3) * 64 + rho * 8;
    int redw = (j & 1) == (n >> 3) ? L_RED + ((tw * 2 + (j & 1)) * 2 + (j >> 1)) * 256 + (n & 7) * 32 : L_DUMP + 4096 + lane * 32;
    v2u wb[8];
    {
      bool ho[4];
      v4u od[4];
      wait_until(t_0 + TGATE);
#pragma unroll
      for (int i = 0; i < 4; ++i) od[i] = ld_sc1(rs, s2off + i * 8 * 2048);
      SB();
#pragma unroll
      for (int e = 0; e < 8; ++e) {
        wb[e][0] = permsel(wfr[1][e >> 1], wfr[0][e >> 1], e);
        wb[e][1] = permsel(wfr[3][e >> 1], wfr[2][e >> 1], e);
        asm volatile("" : "+v"(wb[e]));
      }
      asm volatile("" : "+v"(oaw), "+v"(oar), "+v"(redw));
      SB();
#pragma unroll
      for (int i = 0; i < 4; ++i) {
        ho[i] = (od[i][1] ^ od[i][3]) == tgx;
        o0[i] = od[i][0]; o1[i] = od[i][1]; o2[i] = od[i][2];
      }
      unsigned spins = 0;
#pragma nounroll
      while (true) {
        const int miss = __builtin_popcountll(__ballot(!ho[0])) + __builtin_popcountll(__ballot(!ho[1])) +
                         __builtin_popcountll(__ballot(!ho[2])) + __builtin_popcountll(__ballot(!ho[3]));
        if (miss == 0) break;
        if (++spins >= SPIN_CAP) { alive = false; break; }
        if (miss > 128) __builtin_amdgcn_s_sleep(6);
        else if (miss > 40) __builtin_amdgcn_s_sleep(2);
        CBAR();
#pragma unroll
        for (int i = 0; i < 4; ++i) od[i] = ld_sc1(rs, ho[i] ? OOB : s2off + i * 8 * 2048);
        SB();
#pragma unroll
        for (int i = 0; i < 4; ++i) {
          o0[i] = ho[i] ? o0[i] : od[i][0];
          o1[i] = ho[i] ? o1[i] : od[i][1];
          o2[i] = ho[i] ? o2[i] : od[i][2];
          ho[i] = ho[i] | ((od[i][1] ^ od[i][3]) == tgx);
        }
      }
    }
    {
      float ls[4], ov[10];
#pragma unroll
      for (int i = 0; i < 4; ++i) ls[i] = __uint_as_float(o1[i] ^ tag);
      float mxb[2];
      {
        auto s0 = __builtin_amdgcn_permlane32_swap(__float_as_uint(fmaxf(ls[0], ls[1])), __float_as_uint(fmaxf(ls[2], ls[3])), false, false);
        float m = fmaxf(__uint_as_float(s0[0]), __uint_as_float(s0[1]));
        auto s1 = __builtin_amdgcn_permlane16_swap(__float_as_uint(m), __float_as_uint(m), false, false);
        m = fmaxf(__uint_as_float(s1[0]), __uint_as_float(s1[1]));
        m = fmaxf(m, dpp<D_ROR8>(m));
        auto s2 = __builtin_amdgcn_permlane32_swap(__float_as_uint(m), __float_as_uint(m), false, false);
        mxb[0] = __uint_as_float(s2[0]);
        mxb[1] = __uint_as_float(s2[1]);
      }
#pragma unroll
      for (int bb = 0; bb < 2; ++bb) {
        const float w0 = __builtin_amdgcn_exp2f(ls[2 * bb] - mxb[bb]), w1 = __builtin_amdgcn_exp2f(ls[2 * bb + 1] - mxb[bb]);
        ov[4 * bb + 0] = w0 * bflo(o0[2 * bb]) + w1 * bflo(o0[2 * bb + 1]);
        ov[4 * bb + 1] = w0 * bfhi(o0[2 * bb]) + w1 * bfhi(o0[2 * bb + 1]);
        ov[4 * bb + 2] = w0 * bflo(o2[2 * bb]) + w1 * bflo(o2[2 * bb + 1]);
        ov[4 * bb + 3] = w0 * bfhi(o2[2 * bb]) + w1 * bfhi(o2[2 * bb + 1]);
        ov[8 + bb] = w0 + w1;
      }
#pragma unroll
      for (int i = 0; i < 4; ++i) ov[i] = swapsum32(ov[i], ov[i + 4]);
      float W = swapsum32(ov[8], ov[9]);
      ov[0] = swapsum16(ov[0], ov[2]);
      ov[1] = swapsum16(ov[1], ov[3]);
      W = swapsum16(W, W);
      ov[0] += dpp<D_ROR8>(ov[0]);
      ov[1] += dpp<D_ROR8>(ov[1]);
      W += dpp<D_ROR8>(W);
      float sc = gate * __builtin_amdgcn_rcpf(W);
      if (!alive) sc = __uint_as_float(0x7FC00000u);
      *(LAS unsigned*)(L + oaw) = cvtpk(sc * ov[0], sc * ov[1]);
      CBAR();
      const v2u ao = *(LAS v2u*)(L + oar);
      float res[8];
#pragma unroll
      for (int e = 0; e < 8; ++e) res[e] = mfma16(ao, wb[e], zero4)[0];
      *(LAS f32x4*)(L + redw) = f32x4{res[0], res[1], res[2], res[3]};
      *(LAS f32x4*)(L + redw + 16) = f32x4{res[4], res[5], res[6], res[7]};
    }
    WGBAR();
    if (tw == 0) {
      f32x4 a = zero4;
#pragma unroll
      for (int t = 0; t < 5; ++t) a += *(LAS f32x4*)(L + L_RED + ((lane >> 5) * 5 + t) * 512 + (lane & 31) * 16);
      const f32x4 s = {swapsum32(a[0], a[0]), swapsum32(a[1], a[1]), swapsum32(a[2], a[2]), swapsum32(a[3], a[3])};
      if (lane < 32) st_sc1(rs, S3_OFF + (cblk * 32 + 4 * gc + hrel) * 512 + lane * 16, pack4(s, tag, tagB));
    } else if (tw == 1 && bid < 32) {
      v4u pd[4];
      unsigned spins = 0;
      bool done;
#pragma nounroll
      do {
        CBAR();
#pragma unroll
        for (int i = 0; i < 4; ++i) pd[i] = ld_sc1(rs, s3off + i * 4096);
        SB();
        done = __all(BAD4(pd) == 0u);
      } while (!done && ++spins < SPIN_CAP);
      alive = alive && done;
      const f32x4 a = USUM4(pd);
      float b0 = swapsum32(a[0], a[2]), b1 = swapsum32(a[1], a[3]);
      b0 = swapsum16(b0, b0);
      b1 = swapsum16(b1, b1);
      b0 += dpp<D_ROR8>(b0);
      b1 += dpp<D_ROR8>(b1);
      unsigned pk = cvtpk(b0, b1);
      if (!alive) pk = 0x7FC07FC0u;
      if ((lane & 24) == 0)
        __builtin_amdgcn_raw_buffer_store_b32(pk, __builtin_amdgcn_make_buffer_rsrc(out, 0, 2048, 0x00020000),
                                              (((bid >> 1) & 1) * DM + 64 * (bid >> 2) + 32 * (bid & 1) + 4 * (lane & 7) + 2 * (lane >> 5)) * 2, 0, 0);
      if (bid == 0 && lane == 0) __hip_atomic_store((GAS unsigned*)(ws + EPOCH_OFF), tag, RLX_AGENT);
    }
  }
}

}

extern "C" void kernel_launch(void* const* d_in, const int* in_sizes, int n_in, void* d_out, int out_size, void* d_ws,
                              size_t ws_size, hipStream_t stream) {
  using gm::u16;
  (void)in_sizes; (void)n_in; (void)out_size; (void)ws_size;
  gm::fused_kernel<<<gm::NWG, gm::NTHR, 0, stream>>>(
      (const u16*)d_in[0], (const u16*)d_in[3], (unsigned char*)d_ws, (const u16*)d_in[9], (const u16*)d_in[1],
      (const u16*)d_in[6], (const u16*)d_in[7], (const u16*)d_in[2], (const u16*)d_in[4], (const u16*)d_in[8], (u16*)d_out);
}
```
